```python
import torch

def _softmax(x, axis=-1):
    return torch.softmax(x, dim=axis)
def _gelu(x):
    return torch.nn.functional.gelu(x, approximate="tanh")
def _silu(x):
    return torch.nn.functional.silu(x)
def _sigmoid(x):
    return torch.sigmoid(x)
def _layernorm(x, axis=-1, eps=1e-5):
    m = x.mean(dim=axis, keepdim=True)
    v = x.var(dim=axis, keepdim=True, unbiased=False)
    return (x - m) / torch.sqrt(v + eps)
def _scatter_add(tgt, idx, src, axis=0):
    return tgt.clone().index_add_(axis, idx, src)
def _segment_sum(src, seg, n):
    out = torch.zeros((n,) + src.shape[1:], dtype=src.dtype)
    return out.index_add_(0, seg, src)
def _pad(x, pw):
    flat = tuple(v for pair in reversed(pw) for v in pair)
    return torch.nn.functional.pad(x, flat)

kernel_name = 'standalone__gqa_block_cached__decode'

def setup_inputs():

    _w = (lambda a: (lambda t: t.to(torch.bfloat16) if t.is_floating_point() else t)(torch.as_tensor(a)))
    B, Sq, D = 8, 1, 512
    Hq, Hkv, Sk, Dh = 32, 8, 4096, 128
    assert Hq % Hkv == 0, (Hq, Hkv)
    return {
        "x": _w((lambda *s: torch.randn(*s, dtype=torch.bfloat16) * 0.8)(B, Sq, D)),
        "k_cache": _w((lambda *s: torch.randn(*s, dtype=torch.bfloat16) * 0.8)(B, Hkv, Sk, Dh)),
        "v_cache": _w((lambda *s: torch.randn(*s, dtype=torch.bfloat16) * 0.05)(B, Hkv, Sk, Dh)),
        "wq": _w((lambda *s: torch.randn(*s, dtype=torch.bfloat16) * 0.8)(D, Hq * Dh) * (D ** -0.5)),
        "wo": _w((lambda *s: torch.randn(*s, dtype=torch.bfloat16) * 0.05)(Hq * Dh, D) * ((Hq * Dh) ** -0.5)),
    }

def reference(x, k_cache, v_cache, wq, wo):
    B, Sq, D = x.shape
    Hq, Dh = 32, 128
    Hkv = k_cache.shape[1]
    rep = Hq // Hkv
    q = (lambda x, *d: x.permute(*d))((x @ wq).reshape(B, Sq, Hq, Dh), 0, 2, 1, 3)
    k = (lambda x, n, axis: torch.repeat_interleave(x, n, dim=axis))(k_cache, rep, axis=1)
    v = (lambda x, n, axis: torch.repeat_interleave(x, n, dim=axis))(v_cache, rep, axis=1)
    scores = torch.einsum("bhqd,bhkd->bhqk", q, k) * (Dh ** -0.5)
    p = _softmax(scores, axis=-1)
    o = torch.einsum("bhqk,bhkd->bhqd", p, v)
    return (lambda x, *d: x.permute(*d))(o, 0, 2, 1, 3).reshape(B, Sq, Hq * Dh) @ wo
```

```cpp
#include <hip/hip_runtime.h>
#include <hip/hip_bf16.h>
#include <stdint.h>

typedef unsigned u32;
typedef unsigned short u16;
typedef u32 u32x4 __attribute__((ext_vector_type(4)));
typedef u32 u32x2 __attribute__((ext_vector_type(2)));
typedef float f32x4 __attribute__((ext_vector_type(4)));
typedef __bf16 bf16x8 __attribute__((ext_vector_type(8)));
typedef __bf16 bf16x2 __attribute__((ext_vector_type(2)));
typedef float f32x2 __attribute__((ext_vector_type(2)));

#define KV_AUX 2
#define GQA_STAMP 0

constexpr int NB = 8, DM = 512, HQ = 32, HKV = 8, SK = 4096, DH = 128;
constexpr int AUX_SC1 = 16;
constexpr int AUX_POLL = 16;
constexpr u32 RSRC_W3 = 0x00020000u;
constexpr u32 SPIN_CAP = 1u << 16;

constexpr u32 WS_EPOCH = 0;
constexpr u32 WS_QG = 4096;
constexpr u32 WS_ML = WS_QG + NB * 2048 * 8;
constexpr u32 WS_OG = WS_ML + NB * HKV * 16 * 16;
constexpr u32 WS_PG = WS_OG + NB * HKV * 16 * 128 * 8;
constexpr u32 WS_ST = WS_PG + NB * HQ * DM * 8;
constexpr u32 WS_BYTES = WS_ST + 256 * 8 * 16 * 8;

constexpr int L_QRED = 0;
constexpr int L_MAX = L_QRED + 8 * 128;
constexpr int L_SUM = L_MAX + 32;
constexpr int L_O = L_SUM + 32;
constexpr int L_ACC = L_O + 64;
constexpr int L_FLOATS = L_ACC + 8 * 4 * 132;

__device__ __forceinline__ f32x4 mfma16(u32x4 a, u32x4 b, f32x4 c) {
  return __builtin_amdgcn_mfma_f32_16x16x32_bf16(__builtin_bit_cast(bf16x8, a), __builtin_bit_cast(bf16x8, b), c, 0, 0, 0);
}
__device__ __forceinline__ u32 pack_bf16(float lo, float hi) {
  f32x2 f = {lo, hi};
  return __builtin_bit_cast(u32, __builtin_convertvector(f, bf16x2));
}
__device__ __forceinline__ float xg_max(float x) {
  auto s = __builtin_amdgcn_permlane16_swap(__float_as_uint(x), __float_as_uint(x), false, false);
  x = fmaxf(__uint_as_float(s[0]), __uint_as_float(s[1]));
  auto t = __builtin_amdgcn_permlane32_swap(__float_as_uint(x), __float_as_uint(x), false, false);
  return fmaxf(__uint_as_float(t[0]), __uint_as_float(t[1]));
}
__device__ __forceinline__ float xg_sum(float x) {
  auto s = __builtin_amdgcn_permlane16_swap(__float_as_uint(x), __float_as_uint(x), false, false);
  x = __uint_as_float(s[0]) + __uint_as_float(s[1]);
  auto t = __builtin_amdgcn_permlane32_swap(__float_as_uint(x), __float_as_uint(x), false, false);
  return __uint_as_float(t[0]) + __uint_as_float(t[1]);
}
template <int CTRL> __device__ __forceinline__ float dppf(float x) {
  return __builtin_bit_cast(float, __builtin_amdgcn_mov_dpp(__builtin_bit_cast(int, x), CTRL, 0xf, 0xf, true));
}
#define SB() __builtin_amdgcn_sched_barrier(0)
#define BAR() do { asm volatile("s_waitcnt lgkmcnt(0)" ::: "memory"); __builtin_amdgcn_s_barrier(); asm volatile("" ::: "memory"); } while (0)
#define LD128(rs, vo, so, aux) __builtin_amdgcn_raw_buffer_load_b128((rs), (vo), (so), (aux))
#define LD64(rs, vo, so, aux) __builtin_amdgcn_raw_buffer_load_b64((rs), (vo), (so), (aux))
#define LD32(rs, vo, so, aux) __builtin_amdgcn_raw_buffer_load_b32((rs), (vo), (so), (aux))
#define LD16(rs, vo, so, aux) __builtin_amdgcn_raw_buffer_load_b16((rs), (vo), (so), (aux))

#define STAMP(i) do { } while (0)

__global__ void __launch_bounds__(512, 2)
gqa_decode_fused(const u16* __restrict__ x, const u16* __restrict__ kc, const u16* __restrict__ vc, const u16* __restrict__ wq,
                 const u16* __restrict__ wo, u16* __restrict__ out, unsigned char* ws) {
  __shared__ __attribute__((aligned(16))) float lds[L_FLOATS];
  const int tid = threadIdx.x, lane = tid & 63, w = __builtin_amdgcn_readfirstlane(tid >> 6);
  const int g = lane >> 4, c = lane & 15;
  const int j = blockIdx.x, hkv = j & 7, r = j >> 3, b = r >> 2, s = r & 3;
  STAMP(0);
  const __amdgpu_buffer_rsrc_t rx = __builtin_amdgcn_make_buffer_rsrc((void*)x, 0, NB * DM * 2, RSRC_W3);
  const __amdgpu_buffer_rsrc_t rk = __builtin_amdgcn_make_buffer_rsrc((void*)kc, 0, NB * HKV * SK * DH * 2, RSRC_W3);
  const __amdgpu_buffer_rsrc_t rv = __builtin_amdgcn_make_buffer_rsrc((void*)vc, 0, NB * HKV * SK * DH * 2, RSRC_W3);
  const __amdgpu_buffer_rsrc_t rq = __builtin_amdgcn_make_buffer_rsrc((void*)wq, 0, DM * HQ * DH * 2, RSRC_W3);
  const __amdgpu_buffer_rsrc_t ro = __builtin_amdgcn_make_buffer_rsrc((void*)wo, 0, DM * HQ * DH * 2, RSRC_W3);
  const __amdgpu_buffer_rsrc_t rout = __builtin_amdgcn_make_buffer_rsrc((void*)out, 0, NB * DM * 2, RSRC_W3);
  const __amdgpu_buffer_rsrc_t rw = __builtin_amdgcn_make_buffer_rsrc((void*)ws, 0, WS_BYTES, RSRC_W3);

  const u32 kvbase = (u32)(((b * HKV + hkv) * SK + s * 1024 + w * 128) * (DH * 2));
  const u32 koff = (u32)((4 * (c & 3) + (c >> 2)) * 256 + g * 16);
  u32x4 K[8][4];
#define LOADK(t) _Pragma("unroll") for (int kk = 0; kk < 4; ++kk) K[t][kk] = LD128(rk, koff + kk * 64, kvbase + (t) * 4096, KV_AUX)

  LOADK(0);
  SB();
  u32 vz; asm volatile("v_mov_b32 %0, 0" : "=v"(vz));
  const u32 epoch = LD32(rw, vz + WS_EPOCH, 0, AUX_SC1);
  u32x4 xa[2];
  u32 wl[2][8];
  {
    const u32 xo = (u32)(((c & 7) * DM + 64 * w + 8 * g) * 2);
    const u32 qo = (u32)(((64 * w + 8 * g) * (HQ * DH) + hkv * 512 + 16 * r + c) * 2);
#pragma unroll
    for (int e = 0; e < 2; ++e) {
      xa[e] = LD128(rx, xo + e * 64, 0, 0);
#pragma unroll
      for (int jj = 0; jj < 8; ++jj) wl[e][jj] = (u32)LD16(rq, qo, (32 * e + jj) * (HQ * DH * 2), 0);
    }
  }
  SB();
  LOADK(1); SB(); LOADK(2);
  SB();
  {
    f32x4 qa = {0.f, 0.f, 0.f, 0.f};
#pragma unroll
    for (int e = 0; e < 2; ++e) {
      u32x4 bf;
#pragma unroll
      for (int p = 0; p < 4; ++p) bf[p] = (wl[e][2 * p] & 0xffffu) | (wl[e][2 * p + 1] << 16);
      qa = mfma16(xa[e], bf, qa);
    }
    if (g < 2) *reinterpret_cast<f32x4*>(lds + L_QRED + w * 128 + (g * 16 + c) * 4) = qa;
  }
  STAMP(1);
  BAR();
  u32 tag = epoch * 0x9E3779B1u + 0x7F4A7C15u;
  tag ^= tag >> 15; tag *= 0x85EBCA77u; tag ^= tag >> 13;
  tag = __builtin_amdgcn_readfirstlane(tag);
  if (w == 0) {
    const int half = lane >> 5, l32 = lane & 31;
    f32x4 a = *reinterpret_cast<const f32x4*>(lds + L_QRED + (4 * half + 0) * 128 + l32 * 4);
#pragma unroll
    for (int ww = 1; ww < 4; ++ww) a += *reinterpret_cast<const f32x4*>(lds + L_QRED + (4 * half + ww) * 128 + l32 * 4);
    float sum[4];
#pragma unroll
    for (int i = 0; i < 4; ++i) {
      auto t = __builtin_amdgcn_permlane32_swap(__float_as_uint(a[i]), __float_as_uint(a[i]), false, false);
      sum[i] = __uint_as_float(t[0]) + __uint_as_float(t[1]);
    }
    const int odd = c & 1;
    const float s_lo = half ? sum[2] : sum[0], s_hi = half ? sum[3] : sum[1];
    const float own = odd ? s_hi : s_lo, send = odd ? s_lo : s_hi;
    const float other = dppf<0xB1>(send);
    const u32 v = odd ? pack_bf16(other, own) : pack_bf16(own, other);
    const int bb = 4 * (l32 >> 4) + 2 * half + odd;
    u32x2 gr = {v, tag};
    __builtin_amdgcn_raw_buffer_store_b64(gr, rw, WS_QG + (u32)((bb * 2048 + hkv * 256 + 8 * r + (c >> 1)) * 8), 0, AUX_SC1);
  }
  BAR();
  STAMP(2);
  LOADK(3); SB(); LOADK(4); SB(); LOADK(5); SB(); LOADK(6); SB(); LOADK(7);
  SB();
  asm volatile("s_waitcnt vmcnt(16)" ::: "memory");
  STAMP(3);
  const u32 qoff = WS_QG + (u32)((b * 2048 + hkv * 256 + (c & 3) * 64 + (c >> 2) * 16 + g * 4) * 8);
  u32x4 qa = LD128(rw, qoff, 0, AUX_POLL), qb = LD128(rw, qoff + 16, 0, AUX_POLL);
  SB();
  u32x4 V[32];
  const u32 voff = (u32)(lane * 16);
#define LOADV(n) V[n] = LD128(rv, voff + ((n) & 3) * 1024, kvbase + ((n) >> 2) * 4096, KV_AUX)
#pragma unroll
  for (int n = 0; n < 16; ++n) LOADV(n);
  SB();
  {
    bool ok = (qa[1] == tag) & (qa[3] == tag) & (qb[1] == tag) & (qb[3] == tag);
    if (!__all(ok)) {
      for (u32 spin = 0; spin < SPIN_CAP; ++spin) {
        asm volatile("" ::: "memory");
        qa = LD128(rw, qoff, 0, AUX_POLL); qb = LD128(rw, qoff + 16, 0, AUX_POLL);
        ok = (qa[1] == tag) & (qa[3] == tag) & (qb[1] == tag) & (qb[3] == tag);
        if (__all(ok)) break;
      }
    }
  }
  STAMP(4);
  u32x4 Qf[4];
  {
    const u32 q0[4] = {qa[0], qa[2], qb[0], qb[2]};
#pragma unroll
    for (int kk = 0; kk < 4; ++kk)
#pragma unroll
      for (int e = 0; e < 4; ++e) Qf[kk][e] = (u32)__builtin_amdgcn_ds_bpermute(((lane & 48) | (4 * kk + (lane & 3))) * 4, (int)q0[e]);
  }
  f32x4 S[8];
#pragma unroll
  for (int t = 0; t < 8; ++t) {
    S[t] = f32x4{0.f, 0.f, 0.f, 0.f};
#pragma unroll
    for (int kk = 0; kk < 4; ++kk) S[t] = mfma16(K[t][kk], Qf[kk], S[t]);
  }
  SB();
#pragma unroll
  for (int n = 16; n < 32; ++n) LOADV(n);
  SB();
  float mx = fmaxf(fmaxf(S[0][0], S[0][1]), fmaxf(S[0][2], S[0][3]));
#pragma unroll
  for (int t = 1; t < 8; ++t) mx = fmaxf(mx, fmaxf(fmaxf(S[t][0], S[t][1]), fmaxf(S[t][2], S[t][3])));
  mx = xg_max(mx);
  if (lane < 4) lds[L_MAX + lane * 8 + w] = mx;
  STAMP(5);
  BAR();
  {
    const f32x4 m0 = *reinterpret_cast<const f32x4*>(lds + L_MAX + (c & 3) * 8), m1 = *reinterpret_cast<const f32x4*>(lds + L_MAX + (c & 3) * 8 + 4);
    mx = fmaxf(fmaxf(fmaxf(m0[0], m0[1]), fmaxf(m0[2], m0[3])), fmaxf(fmaxf(m1[0], m1[1]), fmaxf(m1[2], m1[3])));
  }
  constexpr float C2 = 0.088388347648318440f * 1.4426950408889634f;
  const float mneg = -mx * C2;
  float lsum = 0.f;
  u32x4 P[4];
#pragma unroll
  for (int t = 0; t < 8; ++t) {
    float p[4];
#pragma unroll
    for (int i = 0; i < 4; ++i) { p[i] = __builtin_amdgcn_exp2f(fmaf(S[t][i], C2, mneg)); lsum += p[i]; }
    P[t >> 1][(t & 1) * 2 + 0] = pack_bf16(p[0], p[1]);
    P[t >> 1][(t & 1) * 2 + 1] = pack_bf16(p[2], p[3]);
  }
  lsum = xg_sum(lsum);
  if (lane < 4) lds[L_SUM + lane * 8 + w] = lsum;
  f32x4 acc[8];
#pragma unroll
  for (int i = 0; i < 8; ++i) acc[i] = f32x4{0.f, 0.f, 0.f, 0.f};
#define PVSTEP(u) _Pragma("unroll") for (int i = 0; i < 8; ++i) { u32x4 a_; \
    _Pragma("unroll") for (int p = 0; p < 4; ++p) a_[p] = __builtin_amdgcn_perm(V[8 * (u) + 2 * p + 1][i >> 1], V[8 * (u) + 2 * p][i >> 1], (i & 1) ? 0x07060302u : 0x05040100u); \
    acc[i] = mfma16(a_, P[u], acc[i]); }
  PVSTEP(0); SB();
  PVSTEP(1); SB();
  STAMP(6);
  u32x2 W[4][8];
  {
    const u32 wof = (u32)(((64 * (g & 1) + (g >> 1)) * DM + 4 * c) * 2);
    const u32 wob = (u32)((((hkv * 4 + s) * DH) * DM + 64 * w) * 2);
#pragma unroll
    for (int ks = 0; ks < 4; ++ks)
#pragma unroll
      for (int jj = 0; jj < 8; ++jj) W[ks][jj] = LD64(ro, wof + 2 * ks * (DM * 2), wob + 8 * jj * (DM * 2), 0);
  }
  SB();
  BAR();
  if (w == 0 && lane < 4) {
    const f32x4 a0 = *reinterpret_cast<const f32x4*>(lds + L_SUM + lane * 8), a1 = *reinterpret_cast<const f32x4*>(lds + L_SUM + lane * 8 + 4);
    const float L = ((a0[0] + a0[1]) + (a0[2] + a0[3])) + ((a1[0] + a1[1]) + (a1[2] + a1[3]));
    u32x4 gr = {__float_as_uint(mx * C2), tag, __float_as_uint(L), tag};
    __builtin_amdgcn_raw_buffer_store_b128(gr, rw, WS_ML + (u32)((((b * HKV + hkv) * 4 + lane) * 4 + s) * 16), 0, AUX_SC1);
  }
  SB();
  PVSTEP(2); SB();
  PVSTEP(3); SB();
  STAMP(7);
  if (c < 4) {
#pragma unroll
    for (int i = 0; i < 8; ++i) *reinterpret_cast<f32x4*>(lds + L_ACC + (w * 4 + c) * 132 + i * 16 + g * 4) = acc[i];
  }
  u32x4 Wf[4][4];
#define MAKE_WF() do { _Pragma("unroll") for (int ks = 0; ks < 4; ++ks) _Pragma("unroll") for (int i = 0; i < 4; ++i) { _Pragma("unroll") for (int p = 0; p < 4; ++p) \
      Wf[ks][i][p] = __builtin_amdgcn_perm(W[ks][2 * p + 1][i >> 1], W[ks][2 * p][i >> 1], (i & 1) ? 0x07060302u : 0x05040100u); \
      asm volatile("" : "+v"(Wf[ks][i])); } } while (0)
  BAR();
  {
    const int hh = tid >> 7, dl = tid & 127;
    float o = lds[L_ACC + hh * 132 + dl];
#pragma unroll
    for (int ww = 1; ww < 8; ++ww) o += lds[L_ACC + (ww * 4 + hh) * 132 + dl];
    const u32 ogb = WS_OG + (u32)(((b * HKV + hkv) * 4 * 4 * 128) * 8);
    if ((w >> 1) != s) {
      u32x2 gr = {__float_as_uint(o), tag};
      __builtin_amdgcn_raw_buffer_store_b64(gr, rw, ogb + (u32)(((hh * 4 + s) * 128 + dl) * 8), 0, AUX_SC1);
      STAMP(8);
      SB();
      MAKE_WF();
    } else {
      STAMP(8);
      MAKE_WF();
      SB();
      const u32 go = ogb + (u32)((s * 4 * 128 + dl) * 8);
      const u32 mo = WS_ML + (u32)((((b * HKV + hkv) * 4 + s) * 4 + ((s + 1 + lane) & 3)) * 16);
      u32x2 pg[3];
      u32x4 ml;
      for (u32 spin = 0; spin < SPIN_CAP; ++spin) {
        asm volatile("" ::: "memory");
        ml = LD128(rw, mo, 0, AUX_POLL);
#pragma unroll
        for (int e = 0; e < 3; ++e) pg[e] = LD64(rw, go, (u32)(((s + 1 + e) & 3) * 1024), AUX_POLL);
        const bool ok = (ml[1] == tag) & (ml[3] == tag) & (pg[0][1] == tag) & (pg[1][1] == tag) & (pg[2][1] == tag);
        if (__all(ok)) break;
      }
      const float ms = __uint_as_float(ml[0]), ls = __uint_as_float(ml[2]);
      float M = fmaxf(ms, dppf<0xB1>(ms)); M = fmaxf(M, dppf<0x4E>(M));
      const float f = __builtin_amdgcn_exp2f(ms - M);
      const float fl = f * ls;
      const float f0 = dppf<0x00>(f), f1 = dppf<0x55>(f), f2 = dppf<0xAA>(f), f3 = dppf<0xFF>(f);
      const float L = (dppf<0x00>(fl) + dppf<0x55>(fl)) + (dppf<0xAA>(fl) + dppf<0xFF>(fl));
      const float ov = ((f0 * __uint_as_float(pg[0][0]) + f1 * __uint_as_float(pg[1][0])) + (f2 * __uint_as_float(pg[2][0]) + f3 * o)) / L;
      const float nb = dppf<0xB1>(ov);
      if (!(lane & 1)) reinterpret_cast<u32*>(lds + L_O)[dl >> 1] = pack_bf16(ov, nb);
    }
  }
  STAMP(9);
  BAR();
  {
    f32x4 d[4];
#pragma unroll
    for (int i = 0; i < 4; ++i) d[i] = f32x4{0.f, 0.f, 0.f, 0.f};
#pragma unroll
    for (int ks = 0; ks < 4; ++ks) {
      const u32x4 of = *reinterpret_cast<const u32x4*>(lds + L_O + ks * 16 + g * 4);
#pragma unroll
      for (int i = 0; i < 4; ++i) d[i] = mfma16(of, Wf[ks][i], d[i]);
    }
    const float v01 = (g & 1) ? d[1][0] : d[0][0], v23 = (g & 1) ? d[3][0] : d[2][0];
    const float v = (g & 2) ? v23 : v01;
    u32x2 gr = {__float_as_uint(v), tag};
    __builtin_amdgcn_raw_buffer_store_b64(gr, rw, WS_PG + (u32)(((b * HQ + hkv * 4 + s) * DM + 64 * w + 4 * c + g) * 8), 0, AUX_SC1);
  }
  STAMP(10);
  if (w == 0) {
    const int rr = hkv * 4 + s, hh = lane >> 3, cp = lane & 7;
    const u32 po = WS_PG + (u32)(((b * HQ + hh) * DM + 16 * rr + 2 * cp) * 8);
    u32x4 pv[4];
    for (u32 spin = 0; spin < SPIN_CAP; ++spin) {
      asm volatile("" ::: "memory");
      bool ok = true;
#pragma unroll
      for (int i = 0; i < 4; ++i) { pv[i] = LD128(rw, po, (u32)(8 * i * DM * 8), AUX_POLL); }
#pragma unroll
      for (int i = 0; i < 4; ++i) ok &= (pv[i][1] == tag) & (pv[i][3] == tag);
      if (__all(ok)) break;
    }
    float a0 = (__uint_as_float(pv[0][0]) + __uint_as_float(pv[1][0])) + (__uint_as_float(pv[2][0]) + __uint_as_float(pv[3][0]));
    float a1 = (__uint_as_float(pv[0][2]) + __uint_as_float(pv[1][2])) + (__uint_as_float(pv[2][2]) + __uint_as_float(pv[3][2]));
    a0 += dppf<0x128>(a0); a1 += dppf<0x128>(a1);
    a0 = xg_sum(a0); a1 = xg_sum(a1);
    if (lane < 8) __builtin_amdgcn_raw_buffer_store_b32(pack_bf16(a0, a1), rout, (u32)((b * DM + 16 * rr + 2 * cp) * 2), 0, 0);
    if (j == 0 && lane == 0) __builtin_amdgcn_raw_buffer_store_b32(epoch + 1u, rw, WS_EPOCH, 0, AUX_SC1);
  }
  STAMP(11);
}

extern "C" void kernel_launch(void* const* d_in, const int* in_sizes, int n_in, void* d_out, int out_size, void* d_ws, size_t ws_size,
                              hipStream_t stream) {
  (void)in_sizes; (void)n_in; (void)out_size; (void)ws_size;
  hipLaunchKernelGGL(gqa_decode_fused, dim3(256), dim3(512), 0, stream, (const u16*)d_in[0], (const u16*)d_in[1], (const u16*)d_in[2],
                     (const u16*)d_in[3], (const u16*)d_in[4], (u16*)d_out, (unsigned char*)d_ws);
}
```

```cpp
#include <hip/hip_runtime.h>
#include <hip/hip_bf16.h>
#include <stdint.h>

typedef unsigned u32;
typedef unsigned short u16;
typedef u32 u32x4 __attribute__((ext_vector_type(4)));
typedef u32 u32x2 __attribute__((ext_vector_type(2)));
typedef float f32x4 __attribute__((ext_vector_type(4)));
typedef float f32x2 __attribute__((ext_vector_type(2)));
typedef __bf16 bf16x8 __attribute__((ext_vector_type(8)));
typedef __bf16 bf16x2 __attribute__((ext_vector_type(2)));
typedef short s16x4 __attribute__((ext_vector_type(4)));
#define LAS __attribute__((address_space(3)))

#define KV_AUX 2
#define WQ_AUX 2
#define WO_AUX 2
#define WIN 9
#define NKB 11
#define CFAST 8
#define CSLEEP 40
#define ABL 0
#define QDELAY 16
#define GQA_STAMP 0

constexpr int NB = 8, DM = 512, HQ = 32, HKV = 8, SK = 4096, DH = 128;
constexpr int AUX_SC1 = 16;
constexpr u32 RSRC_W3 = 0x00020000u;
constexpr u32 SPIN_CAP = 1u << 16;

constexpr u32 WS_EPOCH = 0;
constexpr u32 WS_QG = 4096;
constexpr u32 WS_ML = WS_QG + NB * HKV * 4 * 512 * 8;
constexpr u32 WS_OG = WS_ML + NB * HKV * 16 * 16;
constexpr u32 WS_PG = WS_OG + HQ * NB * 4 * 64 * 8;
constexpr u32 WS_ST = WS_PG + NB * HQ * DM * 8;
constexpr u32 WS_BYTES = WS_ST + 256 * 8 * 16 * 8;

constexpr int L_PC = 0;
constexpr int L_Q = L_PC + 4096;
constexpr int L_FLAG = L_Q + 256;
constexpr int L_MAX = L_FLAG + 8;
constexpr int L_SUM = L_MAX + 16;
constexpr int L_FW = L_SUM + 16;
constexpr int L_ACC = L_FW + 16;
constexpr int L_WORDS = L_ACC + 4 * 4 * 132;
constexpr int F_Q = 0, F_SM = 1, F_PV = 2, F_VI = 3, F_PUB = 4;

__device__ __forceinline__ f32x4 mfma16(u32x4 a, u32x4 b, f32x4 c) {
  return __builtin_amdgcn_mfma_f32_16x16x32_bf16(__builtin_bit_cast(bf16x8, a), __builtin_bit_cast(bf16x8, b), c, 0, 0, 0);
}
__device__ __forceinline__ u32 pack_bf16(float lo, float hi) {
  f32x2 f = {lo, hi};
  return __builtin_bit_cast(u32, __builtin_convertvector(f, bf16x2));
}
__device__ __forceinline__ float xg_max(float x) {
  auto s = __builtin_amdgcn_permlane16_swap(__float_as_uint(x), __float_as_uint(x), false, false);
  x = fmaxf(__uint_as_float(s[0]), __uint_as_float(s[1]));
  auto t = __builtin_amdgcn_permlane32_swap(__float_as_uint(x), __float_as_uint(x), false, false);
  return fmaxf(__uint_as_float(t[0]), __uint_as_float(t[1]));
}
__device__ __forceinline__ float xg_sum(float x) {
  auto s = __builtin_amdgcn_permlane16_swap(__float_as_uint(x), __float_as_uint(x), false, false);
  x = __uint_as_float(s[0]) + __uint_as_float(s[1]);
  auto t = __builtin_amdgcn_permlane32_swap(__float_as_uint(x), __float_as_uint(x), false, false);
  return __uint_as_float(t[0]) + __uint_as_float(t[1]);
}
template <int CTRL> __device__ __forceinline__ float dppf(float x) {
  return __builtin_bit_cast(float, __builtin_amdgcn_mov_dpp(__builtin_bit_cast(int, x), CTRL, 0xf, 0xf, true));
}
#define SB() __builtin_amdgcn_sched_barrier(0)
#define MEMBAR() asm volatile("" ::: "memory")
#define BAR() do { asm volatile("s_waitcnt lgkmcnt(0)" ::: "memory"); __builtin_amdgcn_s_barrier(); asm volatile("" ::: "memory"); } while (0)
#define LD128(rs, vo, so, aux) __builtin_amdgcn_raw_buffer_load_b128((rs), (vo), (so), (aux))
#define LD64(rs, vo, so, aux) __builtin_amdgcn_raw_buffer_load_b64((rs), (vo), (so), (aux))
#define LD32(rs, vo, so, aux) __builtin_amdgcn_raw_buffer_load_b32((rs), (vo), (so), (aux))
#define READ_TAG() do { asm volatile("s_load_dword %0, %1, 0x0\n\ts_waitcnt lgkmcnt(0)" : "=s"(epoch) : "s"(ws) : "memory"); \
    tag = epoch * 0x9E3779B1u + 0x7F4A7C15u; tag ^= tag >> 15; tag *= 0x85EBCA77u; tag ^= tag >> 13; } while (0)
#define LDS_ADD(i) do { asm volatile("s_waitcnt lgkmcnt(0)" ::: "memory"); \
    if (lane == 0) (void)__hip_atomic_fetch_add((LAS u32*)(lds + L_FLAG + (i)), 1u, __ATOMIC_RELAXED, __HIP_MEMORY_SCOPE_WORKGROUP); } while (0)
#define LDS_WAIT(i, n, slp) do { _Pragma("unroll 1") for (u32 spin_ = 0; spin_ < (SPIN_CAP << 6); ++spin_) { \
      if (__builtin_amdgcn_readfirstlane(*(volatile LAS u32*)(lds + L_FLAG + (i))) >= (u32)(n)) break; \
      if (slp) __builtin_amdgcn_s_sleep(slp); } MEMBAR(); } while (0)
#define PERM_LO 0x05040100u
#define PERM_HI 0x07060302u

#define STAMP(i) do { } while (0)
#define COUNT(i) do { } while (0)
#define STAMP_OUT() do { } while (0)

template <int N> __device__ __forceinline__ void wait_vm() { asm volatile("s_waitcnt vmcnt(%0)" :: "n"(N) : "memory"); }

__global__ void __launch_bounds__(512, 2)
gqa_decode_fused(const u16* __restrict__ x, const u16* __restrict__ kc, const u16* __restrict__ vc, const u16* __restrict__ wq,
                 const u16* __restrict__ wo, u16* __restrict__ out, unsigned char* ws) {
  __shared__ __attribute__((aligned(16))) u32 lds[L_WORDS];
  float* const ldf = reinterpret_cast<float*>(lds);
  const int tid = threadIdx.x, lane = tid & 63, w = __builtin_amdgcn_readfirstlane(tid >> 6);
  const int g = lane >> 4, c = lane & 15;
  const int j = blockIdx.x, xc = j & 7, r = j >> 3, r2 = r >> 2, s = r & 3;
  STAMP(0);
  asm volatile("" :: "s"(x), "s"(kc), "s"(vc), "s"(wq), "s"(wo), "s"(out), "s"(ws));
  const __amdgpu_buffer_rsrc_t rw = __builtin_amdgcn_make_buffer_rsrc((void*)ws, 0, WS_BYTES, RSRC_W3);
  u32 epoch, tag;

  if (w >= 4) {
    const int u = w & 3;
    const __amdgpu_buffer_rsrc_t rk = __builtin_amdgcn_make_buffer_rsrc((void*)kc, 0, NB * HKV * SK * DH * 2, RSRC_W3);
    const __amdgpu_buffer_rsrc_t rv = __builtin_amdgcn_make_buffer_rsrc((void*)vc, 0, NB * HKV * SK * DH * 2, RSRC_W3);
    const u32 kvbase = (u32)(((xc * HKV + r2) * SK + s * 1024 + u * 256) * (DH * 2));
    const u32 koff = (u32)((4 * (c & 3) + ((c >> 2) & 1)) * 256 + (c >> 3) * 64 + g * 16);
    const u32 voff = (u32)(lane * 16);
    u32x4 K[64], V[64];
#define ISSUE(n) do { if ((n) < 128) { if ((n) >= WIN) wait_vm<WIN - 1>(); \
      if ((n) < 64) K[(n) & 63] = LD128(rk, koff + (((n) >> 1) & 1) * 512 + ((n) & 1) * 128, kvbase + ((n) >> 2) * 4096, KV_AUX); \
      else V[(n) & 63] = LD128(rv, voff + ((n) & 3) * 1024, kvbase + (((n) & 63) >> 2) * 4096, KV_AUX); SB(); } } while (0)
#define ISSUE4(n) do { ISSUE(n); ISSUE((n) + 1); ISSUE((n) + 2); ISSUE((n) + 3); } while (0)
    BAR();
#pragma unroll
    for (int n = 0; n < 4 * NKB; ++n) ISSUE(n);
    STAMP(3);
    LDS_WAIT(F_Q, 4, 2);
    STAMP(4);
    u32x4 Qf[2];
#pragma unroll
    for (int ln = 0; ln < 2; ++ln) Qf[ln] = *reinterpret_cast<const u32x4*>(lds + L_Q + (c & 3) * 64 + ln * 32 + ((c >> 2) & 1) * 16 + g * 4);
    f32x4 S[16];
    float mx = -INFINITY;
#define QK(t) do { f32x4 da = mfma16(K[4 * (t)], Qf[0], f32x4{0.f, 0.f, 0.f, 0.f}), db = mfma16(K[4 * (t) + 2], Qf[0], f32x4{0.f, 0.f, 0.f, 0.f}); \
      da = mfma16(K[4 * (t) + 1], Qf[1], da); db = mfma16(K[4 * (t) + 3], Qf[1], db); \
      _Pragma("unroll") for (int e_ = 0; e_ < 4; ++e_) { \
        auto sw = __builtin_amdgcn_permlane32_swap(__float_as_uint(da[e_]), __float_as_uint(db[e_]), false, false); \
        S[t][e_] = __uint_as_float(sw[0]) + dppf<0x104>(__uint_as_float(sw[1])); }     \
      mx = fmaxf(fmaxf(mx, fmaxf(S[t][0], S[t][1])), fmaxf(S[t][2], S[t][3])); SB(); } while (0)
    {
      constexpr int NP = NKB - 4;
      static_assert(2 * NP <= 16 && 4 * NKB + 4 * (NP - 1) - WIN >= 8 * NP, "paired tiles have landed");
#pragma unroll
      for (int i = 0; i < NP; ++i) { QK(2 * i); QK(2 * i + 1); ISSUE4(4 * NKB + 4 * i); }
#pragma unroll
      for (int t = 2 * NP; t < 16; ++t) { QK(t); ISSUE4(4 * NKB + 4 * (t - NP)); }
    }
    constexpr int N1 = 4 * NKB + 4 * (16 - (NKB - 4));
    constexpr float C2 = 0.088388347648318440f * 1.4426950408889634f;
    mx = xg_max(mx) * C2;
    float lsum = 0.f;
    u32x4 P[8];
#pragma unroll
    for (int t = 0; t < 16; ++t) {
      float p[4];
#pragma unroll
      for (int i = 0; i < 4; ++i) { p[i] = __builtin_amdgcn_exp2f(fmaf(S[t][i], C2, -mx)); lsum += p[i]; }
      P[t >> 1][(t & 1) * 2 + 0] = pack_bf16(p[0], p[1]);
      P[t >> 1][(t & 1) * 2 + 1] = pack_bf16(p[2], p[3]);
      if ((t & 3) == 3) { SB(); ISSUE(N1 + (t >> 2)); }
    }
    lsum = xg_sum(lsum);
    if (lane < 4) { ldf[L_MAX + lane * 4 + u] = mx; ldf[L_SUM + lane * 4 + u] = lsum; }
    LDS_ADD(F_SM);
    STAMP(5);
    constexpr int N2 = N1 + 4;
    f32x4 acc[8];
#pragma unroll
    for (int i = 0; i < 8; ++i) acc[i] = f32x4{0.f, 0.f, 0.f, 0.f};
#pragma unroll
    for (int q = 0; q < 8; ++q) {
#pragma unroll
      for (int n = (q == 0 ? N2 : (64 + 8 * (q - 1) + 7 + WIN + 4 > N2 ? 64 + 8 * (q - 1) + 7 + WIN + 4 : N2)); n < 64 + 8 * q + 7 + WIN; ++n) ISSUE(n);
      if (64 + 8 * q + 7 + WIN >= 128 && 64 + 8 * (q - 1) + 7 + WIN < 128) { LDS_ADD(F_VI); READ_TAG(); }
#pragma unroll
      for (int i = 0; i < 8; ++i) {
        u32x4 a_;
#pragma unroll
        for (int p = 0; p < 4; ++p) a_[p] = __builtin_amdgcn_perm(V[8 * q + 2 * p + 1][i >> 1], V[8 * q + 2 * p][i >> 1], (i & 1) ? PERM_HI : PERM_LO);
        acc[i] = mfma16(a_, P[q], acc[i]);
        if ((i & 1) && 64 + 8 * q + 7 + WIN + (i >> 1) >= N2) { SB(); ISSUE(64 + 8 * q + 7 + WIN + (i >> 1)); }
      }
      SB();
    }
    STAMP(6);
    if (c < 4) {
#pragma unroll
      for (int i = 0; i < 8; ++i) *reinterpret_cast<f32x4*>(ldf + L_ACC + (u * 4 + c) * 132 + i * 16 + g * 4) = acc[i];
    }
    const u32 oga = WS_OG + (u32)(((((r2 * 4 + u) * NB + xc) * 4 + s) * 64 + lane) * 8);
    LDS_ADD(F_PV);
    LDS_WAIT(F_PV, 5, 0);
    STAMP(7);
    const f32x4 fa = *reinterpret_cast<const f32x4*>(ldf + L_FW + u * 4);
    f32x2 o[4];
#pragma unroll
    for (int q = 0; q < 4; ++q) o[q] = *reinterpret_cast<const f32x2*>(ldf + L_ACC + (q * 4 + u) * 132 + 2 * lane);
    const f32x2 ov = (fa[0] * o[0] + fa[1] * o[1]) + (fa[2] * o[2] + fa[3] * o[3]);
    u32x2 gr = {pack_bf16(ov[0], ov[1]), tag};
    __builtin_amdgcn_raw_buffer_store_b64(gr, rw, oga, 0, AUX_SC1);
    STAMP(8);
  } else {
    __builtin_amdgcn_s_setprio(2);
    if (w == 0) {
      const int qk = xc & 3, qc = (xc >> 2) * 32 + r;
      const __amdgpu_buffer_rsrc_t rx = __builtin_amdgcn_make_buffer_rsrc((void*)x, 0, NB * DM * 2, RSRC_W3);
      const __amdgpu_buffer_rsrc_t rq = __builtin_amdgcn_make_buffer_rsrc((void*)wq, 0, DM * HQ * DH * 2, RSRC_W3);
      u32x2 wl[4][8];
      u32x4 xa[4];
      const u32 xo = (u32)(((c & 7) * DM + 128 * qk + 8 * g) * 2);
      const u32 qo = (u32)(8 * g * (HQ * DH * 2) + c * 8), qb0 = (u32)((128 * qk * (HQ * DH) + 64 * qc) * 2);
#pragma unroll
      for (int ks = 0; ks < 4; ++ks) {
#pragma unroll
        for (int jj = 0; jj < 8; ++jj) wl[ks][jj] = LD64(rq, qo, qb0 + (32 * ks + jj) * (HQ * DH * 2), WQ_AUX);
        xa[ks] = LD128(rx, xo + ks * 64, 0, 0);
      }
      *reinterpret_cast<u32x4*>(lds + L_FLAG) = u32x4{0u, 0u, 0u, 0u};
      *reinterpret_cast<u32x4*>(lds + L_FLAG + 4) = u32x4{0u, 0u, 0u, 0u};
      SB();
      BAR();
      READ_TAG();
      SB();
      f32x4 d[4];
#pragma unroll
      for (int i = 0; i < 4; ++i) d[i] = f32x4{0.f, 0.f, 0.f, 0.f};
#pragma unroll
      for (int ks = 0; ks < 4; ++ks)
#pragma unroll
        for (int i = 0; i < 4; ++i) {
          u32x4 bf;
#pragma unroll
          for (int p = 0; p < 4; ++p) bf[p] = __builtin_amdgcn_perm(wl[ks][2 * p + 1][i >> 1], wl[ks][2 * p][i >> 1], (i & 1) ? PERM_HI : PERM_LO);
          d[i] = mfma16(xa[ks], bf, d[i]);
        }
      const int half = g >> 1;
      const u32 qs = WS_QG + (u32)(((((4 * (g & 1) + 2 * half) * HKV + (qc >> 3)) * 4 + qk) * 512 + (qc & 7) * 64 + 2 * c) * 8);
#pragma unroll
      for (int e = 0; e < 2; ++e)
#pragma unroll
        for (int ih = 0; ih < 2; ++ih) {
          const float v0 = half ? d[2 * ih][2 + e] : d[2 * ih][e], v1 = half ? d[2 * ih + 1][2 + e] : d[2 * ih + 1][e];
          u32x4 gr = {__float_as_uint(v0), tag, __float_as_uint(v1), tag};
          __builtin_amdgcn_raw_buffer_store_b128(gr, rw, qs + ih * 256, e * (HKV * 4 * 512 * 8), AUX_SC1);
        }
      LDS_ADD(F_PUB);
      STAMP(1);
    } else {
      BAR();
      READ_TAG();
      LDS_WAIT(F_PUB, 1, 2);
    }
    __builtin_amdgcn_s_sleep(QDELAY);
    {
      const u32 qoff = WS_QG + (u32)((xc * HKV + r2) * (4 * 512 * 8) + w * 1024 + lane * 16);
      u32x4 ga[4];
#pragma unroll
      for (int kq = 0; kq < 4; ++kq) ga[kq] = LD128(rw, qoff, kq * 4096, AUX_SC1);
#pragma unroll 1
      for (u32 spin = 0; spin < SPIN_CAP; ++spin) {
        bool ok[4];
#pragma unroll
        for (int kq = 0; kq < 4; ++kq) ok[kq] = (ga[kq][1] == tag) & (ga[kq][3] == tag);
        COUNT(15);
        if (__all(ok[0] & ok[1] & ok[2] & ok[3])) break;
        MEMBAR();
#pragma unroll
        for (int kq = 0; kq < 4; ++kq) if (!ok[kq]) ga[kq] = LD128(rw, qoff, kq * 4096, AUX_SC1);
      }
      const float q0 = (__uint_as_float(ga[0][0]) + __uint_as_float(ga[1][0])) + (__uint_as_float(ga[2][0]) + __uint_as_float(ga[3][0]));
      const float q1 = (__uint_as_float(ga[0][2]) + __uint_as_float(ga[1][2])) + (__uint_as_float(ga[2][2]) + __uint_as_float(ga[3][2]));
      lds[L_Q + 64 * w + 32 * (lane >> 5) + 2 * c + (g & 1)] = pack_bf16(q0, q1);
      LDS_ADD(F_Q);
      STAMP(2);
    }
    u32x4 Wf[4];
    {
      const __amdgpu_buffer_rsrc_t ro = __builtin_amdgcn_make_buffer_rsrc((void*)wo, 0, DM * HQ * DH * 2, RSRC_W3);
      const u32 wof = (u32)((32 * g * DM + 4 * c) * 2);
      const u32 wob = (u32)(((r * DH + 2 * w) * DM + 64 * xc) * 2);
      u32x2 W[8];
#pragma unroll
      for (int jj = 0; jj < 8; ++jj) W[jj] = LD64(ro, wof + (jj >> 2) * (DM * 2), wob + 8 * (jj & 3) * (DM * 2), WO_AUX);
#pragma unroll
      for (int i = 0; i < 4; ++i) {
#pragma unroll
        for (int p = 0; p < 4; ++p) Wf[i][p] = __builtin_amdgcn_perm(W[2 * p + 1][i >> 1], W[2 * p][i >> 1], (i & 1) ? PERM_HI : PERM_LO);
        asm volatile("" : "+v"(Wf[i]));
      }
    }
    if (w == 1) {
      LDS_WAIT(F_SM, 4, 8);
      const float mw = ldf[L_MAX + c], lw = ldf[L_SUM + c];
      float M = fmaxf(mw, dppf<0xB1>(mw)); M = fmaxf(M, dppf<0x4E>(M));
      float L = __builtin_amdgcn_exp2f(mw - M) * lw; L += dppf<0xB1>(L); L += dppf<0x4E>(L);
      const u32 mo = WS_ML + (u32)(((xc * HKV + r2) * 16 + c) * 16);
      if (lane < 16 && (lane & 3) == s) __builtin_amdgcn_raw_buffer_store_b128(u32x4{__float_as_uint(M), tag, __float_as_uint(L), tag}, rw, mo, 0, AUX_SC1);
      u32x4 ml;
#pragma unroll 1
      for (u32 spin = 0; spin < SPIN_CAP; ++spin) {
        __builtin_amdgcn_s_sleep(16);
        MEMBAR();
        ml = LD128(rw, mo, 0, AUX_SC1);
        COUNT(12);
        if (__all((ml[1] == tag) & (ml[3] == tag))) break;
      }
      const float ms = __uint_as_float(ml[0]), ls = __uint_as_float(ml[2]);
      float MG = fmaxf(ms, dppf<0xB1>(ms)); MG = fmaxf(MG, dppf<0x4E>(MG));
      float LG = __builtin_amdgcn_exp2f(ms - MG) * ls; LG += dppf<0xB1>(LG); LG += dppf<0x4E>(LG);
      if (lane < 16) ldf[L_FW + lane] = __builtin_amdgcn_exp2f(mw - MG) / LG;
      LDS_ADD(F_PV);
      STAMP(5);
    }
    const u32 go = WS_OG + (u32)((((r * NB + (c & 7)) * 4 + 2 * (c >> 3)) * 64 + 16 * w + 2 * g) * 8);
    const u32 pga = WS_PG + (u32)(((((4 * (lane >> 5) + 2 * (lane & 1)) * 32 + xc * 4 + w) * HQ + r) * 16 + ((lane >> 1) & 15)) * 8);
    const int lo = L_PC + w * 256 + 2 * lane;
    LDS_WAIT(F_VI, 4, 8);
    STAMP(7);
    u32x4 a[2][2];
#pragma unroll
    for (int sp = 0; sp < 2; ++sp) { a[sp][0] = LD128(rw, go + sp * 512, 0, AUX_SC1); a[sp][1] = LD128(rw, go + sp * 512 + 64, 0, AUX_SC1); }
#pragma unroll 1
    for (u32 spin = 0; spin < SPIN_CAP; ++spin) {
      const bool ok0 = (a[0][0][1] == tag) & (a[0][0][3] == tag) & (a[0][1][1] == tag) & (a[0][1][3] == tag);
      const bool ok1 = (a[1][0][1] == tag) & (a[1][0][3] == tag) & (a[1][1][1] == tag) & (a[1][1][3] == tag);
      COUNT(14);
      const int miss = __builtin_popcountll(__ballot(!ok0)) + __builtin_popcountll(__ballot(!ok1));
      if (miss == 0) break;
      STAMP(6);
      if (miss > 4 * CFAST) __builtin_amdgcn_s_sleep(CSLEEP);
      MEMBAR();
      if (!ok0) { a[0][0] = LD128(rw, go, 0, AUX_SC1); a[0][1] = LD128(rw, go + 64, 0, AUX_SC1); }
      if (!ok1) { a[1][0] = LD128(rw, go + 512, 0, AUX_SC1); a[1][1] = LD128(rw, go + 512 + 64, 0, AUX_SC1); }
    }
    STAMP(8);
    const u32x4 A0 = {a[0][0][0], a[0][0][2], a[0][1][0], a[0][1][2]}, A1 = {a[1][0][0], a[1][0][2], a[1][1][0], a[1][1][2]};
#pragma unroll
    for (int i = 0; i < 4; ++i) {
      f32x4 d = mfma16(A0, Wf[i], f32x4{0.f, 0.f, 0.f, 0.f});
      d = mfma16(A1, Wf[i], d);
      *reinterpret_cast<f32x4*>(ldf + L_PC + (w * 4 + i) * 256 + lane * 4) = d;
    }
    BAR();
    f32x2 t0 = {0.f, 0.f}, t1 = {0.f, 0.f};
#pragma unroll
    for (int kq = 0; kq < 4; ++kq) { t0 += *reinterpret_cast<const f32x2*>(ldf + lo + kq * 1024); t1 += *reinterpret_cast<const f32x2*>(ldf + lo + kq * 1024 + 128); }
    t0 += t1;
    __builtin_amdgcn_raw_buffer_store_b64(u32x2{__float_as_uint(t0[0]), tag}, rw, pga, 0, AUX_SC1);
    __builtin_amdgcn_raw_buffer_store_b64(u32x2{__float_as_uint(t0[1]), tag}, rw, pga, 32 * HQ * 16 * 8, AUX_SC1);
    STAMP(10);
    if (w == 0) {
      const __amdgpu_buffer_rsrc_t rout = __builtin_amdgcn_make_buffer_rsrc((void*)out, 0, NB * DM * 2, RSRC_W3);
      const u32 po = WS_PG + (u32)((xc * 32 + r) * (HQ * 16 * 8) + lane * 16);
      u32x4 pv[4];
#pragma unroll
      for (int i = 0; i < 4; ++i) pv[i] = LD128(rw, po + i * 1024, 0, AUX_SC1);
#pragma unroll 1
      for (u32 spin = 0; spin < SPIN_CAP; ++spin) {
        bool ok[4];
#pragma unroll
        for (int i = 0; i < 4; ++i) ok[i] = (pv[i][1] == tag) & (pv[i][3] == tag);
        COUNT(13);
        if (__all(ok[0] & ok[1] & ok[2] & ok[3])) break;
        STAMP(9);
        MEMBAR();
#pragma unroll
        for (int i = 0; i < 4; ++i) if (!ok[i]) pv[i] = LD128(rw, po + i * 1024, 0, AUX_SC1);
      }
      float a0 = (__uint_as_float(pv[0][0]) + __uint_as_float(pv[1][0])) + (__uint_as_float(pv[2][0]) + __uint_as_float(pv[3][0]));
      float a1 = (__uint_as_float(pv[0][2]) + __uint_as_float(pv[1][2])) + (__uint_as_float(pv[2][2]) + __uint_as_float(pv[3][2]));
      a0 += dppf<0x128>(a0); a1 += dppf<0x128>(a1);
      a0 = xg_sum(a0); a1 = xg_sum(a1);
      const u32 pk = pack_bf16(a0, a1);
      const int cc = 2 * (lane & 7) + ((lane >> 3) & 1);
      if (lane < 16) __builtin_amdgcn_raw_buffer_store_b16((u16)((lane & 8) ? (pk >> 16) : pk), rout, (u32)((xc * DM + 64 * r2 + 4 * cc + s) * 2), 0, 0);
      if (j == 0 && lane == 0) __builtin_amdgcn_raw_buffer_store_b32(epoch + 1u, rw, WS_EPOCH, 0, AUX_SC1);
      STAMP(11);
    }
  }
  STAMP_OUT();
}

extern "C" void kernel_launch(void* const* d_in, const int* in_sizes, int n_in, void* d_out, int out_size, void* d_ws, size_t ws_size,
                              hipStream_t stream) {
  (void)in_sizes; (void)n_in; (void)out_size; (void)ws_size;
  hipLaunchKernelGGL(gqa_decode_fused, dim3(256), dim3(512), 0, stream, (const u16*)d_in[0], (const u16*)d_in[1], (const u16*)d_in[2],
                     (const u16*)d_in[3], (const u16*)d_in[4], (u16*)d_out, (unsigned char*)d_ws);
}
```

```cpp
#include <hip/hip_runtime.h>
#include <hip/hip_bf16.h>
#include <stdint.h>

typedef unsigned u32;
typedef unsigned short u16;
typedef u32 u32x4 __attribute__((ext_vector_type(4)));
typedef u32 u32x2 __attribute__((ext_vector_type(2)));
typedef float f32x4 __attribute__((ext_vector_type(4)));
typedef float f32x2 __attribute__((ext_vector_type(2)));
typedef __bf16 bf16x8 __attribute__((ext_vector_type(8)));
typedef __bf16 bf16x2 __attribute__((ext_vector_type(2)));
typedef short s16x4 __attribute__((ext_vector_type(4)));
#define LAS __attribute__((address_space(3)))

#define KV_AUX 2
#define WQ_AUX 2
#define WO_AUX 2
#define WIN 9
#define NKB 11
#define CFAST 8
#define CSLEEP 40
#define ABL 0
#define X_AUX 0
#define QS_AUX 16
#define QL_AUX 16
#define QDELAY 16
#define GQA_STAMP 0

constexpr int NB = 8, DM = 512, HQ = 32, HKV = 8, SK = 4096, DH = 128;
constexpr int AUX_SC1 = 16;
constexpr u32 RSRC_W3 = 0x00020000u;
constexpr u32 SPIN_CAP = 1u << 16;

constexpr u32 WS_EPOCH = 0;
constexpr u32 WS_QG = 4096;
constexpr u32 WS_ML = WS_QG + NB * HKV * 4 * 512 * 8;
constexpr u32 WS_OG = WS_ML + NB * HKV * 16 * 16;
constexpr u32 WS_PG = WS_OG + HQ * NB * 4 * 64 * 8;
constexpr u32 WS_ST = WS_PG + NB * HQ * DM * 8;
constexpr u32 WS_BYTES = WS_ST + 256 * 8 * 16 * 8;

constexpr int L_PC = 0;
constexpr int L_Q = L_PC + 4096;
constexpr int L_FLAG = L_Q + 256;
constexpr int L_MAX = L_FLAG + 8;
constexpr int L_SUM = L_MAX + 16;
constexpr int L_FW = L_SUM + 16;
constexpr int L_ACC = L_FW + 16;
constexpr int L_WORDS = L_ACC + 4 * 4 * 132;
constexpr int F_Q = 0, F_SM = 1, F_PV = 2, F_VI = 3, F_PUB = 4;

__device__ __forceinline__ f32x4 mfma16(u32x4 a, u32x4 b, f32x4 c) {
  return __builtin_amdgcn_mfma_f32_16x16x32_bf16(__builtin_bit_cast(bf16x8, a), __builtin_bit_cast(bf16x8, b), c, 0, 0, 0);
}
__device__ __forceinline__ u32 pack_bf16(float lo, float hi) {
  f32x2 f = {lo, hi};
  return __builtin_bit_cast(u32, __builtin_convertvector(f, bf16x2));
}
__device__ __forceinline__ float xg_max(float x) {
  auto s = __builtin_amdgcn_permlane16_swap(__float_as_uint(x), __float_as_uint(x), false, false);
  x = fmaxf(__uint_as_float(s[0]), __uint_as_float(s[1]));
  auto t = __builtin_amdgcn_permlane32_swap(__float_as_uint(x), __float_as_uint(x), false, false);
  return fmaxf(__uint_as_float(t[0]), __uint_as_float(t[1]));
}
__device__ __forceinline__ float xg_sum(float x) {
  auto s = __builtin_amdgcn_permlane16_swap(__float_as_uint(x), __float_as_uint(x), false, false);
  x = __uint_as_float(s[0]) + __uint_as_float(s[1]);
  auto t = __builtin_amdgcn_permlane32_swap(__float_as_uint(x), __float_as_uint(x), false, false);
  return __uint_as_float(t[0]) + __uint_as_float(t[1]);
}
template <int CTRL> __device__ __forceinline__ float dppf(float x) {
  return __builtin_bit_cast(float, __builtin_amdgcn_mov_dpp(__builtin_bit_cast(int, x), CTRL, 0xf, 0xf, true));
}
#define SB() __builtin_amdgcn_sched_barrier(0)
#define MEMBAR() asm volatile("" ::: "memory")
#define BAR() do { asm volatile("s_waitcnt lgkmcnt(0)" ::: "memory"); __builtin_amdgcn_s_barrier(); asm volatile("" ::: "memory"); } while (0)
#define LD128(rs, vo, so, aux) __builtin_amdgcn_raw_buffer_load_b128((rs), (vo), (so), (aux))
#define LD64(rs, vo, so, aux) __builtin_amdgcn_raw_buffer_load_b64((rs), (vo), (so), (aux))
#define LD32(rs, vo, so, aux) __builtin_amdgcn_raw_buffer_load_b32((rs), (vo), (so), (aux))
#define READ_TAG() do { asm volatile("s_load_dword %0, %1, 0x0\n\ts_waitcnt lgkmcnt(0)" : "=s"(epoch) : "s"(ws) : "memory"); \
    tag = epoch * 0x9E3779B1u + 0x7F4A7C15u; tag ^= tag >> 15; tag *= 0x85EBCA77u; tag ^= tag >> 13; } while (0)
#define LDS_ADD(i) do { asm volatile("s_waitcnt lgkmcnt(0)" ::: "memory"); \
    if (lane == 0) (void)__hip_atomic_fetch_add((LAS u32*)(lds + L_FLAG + (i)), 1u, __ATOMIC_RELAXED, __HIP_MEMORY_SCOPE_WORKGROUP); } while (0)
#define LDS_WAIT(i, n, slp) do { _Pragma("unroll 1") for (u32 spin_ = 0; spin_ < (SPIN_CAP << 6); ++spin_) { \
      if (__builtin_amdgcn_readfirstlane(*(volatile LAS u32*)(lds + L_FLAG + (i))) >= (u32)(n)) break; \
      if (slp) __builtin_amdgcn_s_sleep(slp); } MEMBAR(); } while (0)
#define PERM_LO 0x05040100u
#define PERM_HI 0x07060302u

#define STAMP(i) do { } while (0)
#define COUNT(i) do { } while (0)
#define STAMP_OUT() do { } while (0)

template <int N> __device__ __forceinline__ void wait_vm() { asm volatile("s_waitcnt vmcnt(%0)" :: "n"(N) : "memory"); }

__global__ void __launch_bounds__(512, 2)
gqa_decode_fused(const u16* __restrict__ x, const u16* __restrict__ kc, const u16* __restrict__ vc, const u16* __restrict__ wq,
                 const u16* __restrict__ wo, u16* __restrict__ out, unsigned char* ws) {
  __shared__ __attribute__((aligned(16))) u32 lds[L_WORDS];
  float* const ldf = reinterpret_cast<float*>(lds);
  const int tid = threadIdx.x, lane = tid & 63, w = __builtin_amdgcn_readfirstlane(tid >> 6);
  const int g = lane >> 4, c = lane & 15;
  const int j = blockIdx.x, xc = j & 7, r = j >> 3, r2 = r >> 2, s = r & 3;
  STAMP(0);
  asm volatile("" :: "s"(x), "s"(kc), "s"(vc), "s"(wq), "s"(wo), "s"(out), "s"(ws));
  const __amdgpu_buffer_rsrc_t rw = __builtin_amdgcn_make_buffer_rsrc((void*)ws, 0, WS_BYTES, RSRC_W3);
  u32 epoch, tag;

  if (w >= 4) {
    const int u = w & 3;
    const __amdgpu_buffer_rsrc_t rk = __builtin_amdgcn_make_buffer_rsrc((void*)kc, 0, NB * HKV * SK * DH * 2, RSRC_W3);
    const __amdgpu_buffer_rsrc_t rv = __builtin_amdgcn_make_buffer_rsrc((void*)vc, 0, NB * HKV * SK * DH * 2, RSRC_W3);
    const u32 kvbase = (u32)(((xc * HKV + r2) * SK + s * 1024 + u * 256) * (DH * 2));
    const u32 koff = (u32)((4 * (c & 3) + ((c >> 2) & 1)) * 256 + (c >> 3) * 64 + g * 16);
    const u32 voff = (u32)(lane * 16);
    u32x4 K[64], V[64];
#define ISSUE(n) do { if ((n) < 128) { if ((n) >= WIN) wait_vm<WIN - 1>(); \
      if ((n) < 64) K[(n) & 63] = LD128(rk, koff + (((n) >> 1) & 1) * 512 + ((n) & 1) * 128, kvbase + ((n) >> 2) * 4096, KV_AUX); \
      else V[(n) & 63] = LD128(rv, voff + ((n) & 3) * 1024, kvbase + (((n) & 63) >> 2) * 4096, KV_AUX); SB(); } } while (0)
#define ISSUE4(n) do { ISSUE(n); ISSUE((n) + 1); ISSUE((n) + 2); ISSUE((n) + 3); } while (0)
    BAR();
#pragma unroll
    for (int n = 0; n < 4 * NKB; ++n) ISSUE(n);
    STAMP(3);
    LDS_WAIT(F_Q, 4, 2);
    STAMP(4);
    u32x4 Qf[2];
#pragma unroll
    for (int ln = 0; ln < 2; ++ln) Qf[ln] = *reinterpret_cast<const u32x4*>(lds + L_Q + (c & 3) * 64 + ln * 32 + ((c >> 2) & 1) * 16 + g * 4);
    f32x4 S[16];
    float mx = -INFINITY;
#define QK(t) do { f32x4 da = mfma16(K[4 * (t)], Qf[0], f32x4{0.f, 0.f, 0.f, 0.f}), db = mfma16(K[4 * (t) + 2], Qf[0], f32x4{0.f, 0.f, 0.f, 0.f}); \
      da = mfma16(K[4 * (t) + 1], Qf[1], da); db = mfma16(K[4 * (t) + 3], Qf[1], db); \
      _Pragma("unroll") for (int e_ = 0; e_ < 4; ++e_) { \
        auto sw = __builtin_amdgcn_permlane32_swap(__float_as_uint(da[e_]), __float_as_uint(db[e_]), false, false); \
        S[t][e_] = __uint_as_float(sw[0]) + dppf<0x104>(__uint_as_float(sw[1])); }     \
      mx = fmaxf(fmaxf(mx, fmaxf(S[t][0], S[t][1])), fmaxf(S[t][2], S[t][3])); SB(); } while (0)
    {
      constexpr int NP = NKB - 4;
      static_assert(2 * NP <= 16 && 4 * NKB + 4 * (NP - 1) - WIN >= 8 * NP, "paired tiles have landed");
#pragma unroll
      for (int i = 0; i < NP; ++i) { QK(2 * i); QK(2 * i + 1); ISSUE4(4 * NKB + 4 * i); }
#pragma unroll
      for (int t = 2 * NP; t < 16; ++t) { QK(t); ISSUE4(4 * NKB + 4 * (t - NP)); }
    }
    constexpr int N1 = 4 * NKB + 4 * (16 - (NKB - 4));
    constexpr float C2 = 0.088388347648318440f * 1.4426950408889634f;
    mx = xg_max(mx) * C2;
    float lsum = 0.f;
    u32x4 P[8];
#pragma unroll
    for (int t = 0; t < 16; ++t) {
      float p[4];
#pragma unroll
      for (int i = 0; i < 4; ++i) { p[i] = __builtin_amdgcn_exp2f(fmaf(S[t][i], C2, -mx)); lsum += p[i]; }
      P[t >> 1][(t & 1) * 2 + 0] = pack_bf16(p[0], p[1]);
      P[t >> 1][(t & 1) * 2 + 1] = pack_bf16(p[2], p[3]);
      if ((t & 3) == 3) { SB(); ISSUE(N1 + (t >> 2)); }
    }
    lsum = xg_sum(lsum);
    if (lane < 4) { ldf[L_MAX + lane * 4 + u] = mx; ldf[L_SUM + lane * 4 + u] = lsum; }
    LDS_ADD(F_SM);
    STAMP(5);
    constexpr int N2 = N1 + 4;
    f32x4 acc[8];
#pragma unroll
    for (int i = 0; i < 8; ++i) acc[i] = f32x4{0.f, 0.f, 0.f, 0.f};
#pragma unroll
    for (int q = 0; q < 8; ++q) {
#pragma unroll
      for (int n = (q == 0 ? N2 : (64 + 8 * (q - 1) + 7 + WIN + 4 > N2 ? 64 + 8 * (q - 1) + 7 + WIN + 4 : N2)); n < 64 + 8 * q + 7 + WIN; ++n) ISSUE(n);
      if (64 + 8 * q + 7 + WIN >= 128 && 64 + 8 * (q - 1) + 7 + WIN < 128) { LDS_ADD(F_VI); READ_TAG(); }
#pragma unroll
      for (int i = 0; i < 8; ++i) {
        u32x4 a_;
#pragma unroll
        for (int p = 0; p < 4; ++p) a_[p] = __builtin_amdgcn_perm(V[8 * q + 2 * p + 1][i >> 1], V[8 * q + 2 * p][i >> 1], (i & 1) ? PERM_HI : PERM_LO);
        acc[i] = mfma16(a_, P[q], acc[i]);
        if ((i & 1) && 64 + 8 * q + 7 + WIN + (i >> 1) >= N2) { SB(); ISSUE(64 + 8 * q + 7 + WIN + (i >> 1)); }
      }
      SB();
    }
    STAMP(6);
    if (c < 4) {
#pragma unroll
      for (int i = 0; i < 8; ++i) *reinterpret_cast<f32x4*>(ldf + L_ACC + (u * 4 + c) * 132 + i * 16 + g * 4) = acc[i];
    }
    const u32 oga = WS_OG + (u32)(((((r2 * 4 + u) * NB + xc) * 4 + s) * 64 + lane) * 8);
    LDS_ADD(F_PV);
    LDS_WAIT(F_PV, 5, 0);
    STAMP(7);
    const f32x4 fa = *reinterpret_cast<const f32x4*>(ldf + L_FW + u * 4);
    f32x2 o[4];
#pragma unroll
    for (int q = 0; q < 4; ++q) o[q] = *reinterpret_cast<const f32x2*>(ldf + L_ACC + (q * 4 + u) * 132 + 2 * lane);
    const f32x2 ov = (fa[0] * o[0] + fa[1] * o[1]) + (fa[2] * o[2] + fa[3] * o[3]);
    u32x2 gr = {pack_bf16(ov[0], ov[1]), tag};
    __builtin_amdgcn_raw_buffer_store_b64(gr, rw, oga, 0, AUX_SC1);
    STAMP(8);
  } else {
    __builtin_amdgcn_s_setprio(2);
    if (w == 0) {
      const int qk = xc & 3, qc = (xc >> 2) * 32 + r;
      const __amdgpu_buffer_rsrc_t rx = __builtin_amdgcn_make_buffer_rsrc((void*)x, 0, NB * DM * 2, RSRC_W3);
      const __amdgpu_buffer_rsrc_t rq = __builtin_amdgcn_make_buffer_rsrc((void*)wq, 0, DM * HQ * DH * 2, RSRC_W3);
      u32x2 wl[4][8];
      u32x4 xa[4];
      const u32 xo = (u32)(((c & 7) * DM + 128 * qk + 8 * g) * 2);
      const u32 qo = (u32)(8 * g * (HQ * DH * 2) + c * 8), qb0 = (u32)((128 * qk * (HQ * DH) + 64 * qc) * 2);
#pragma unroll
      for (int ks = 0; ks < 4; ++ks) {
#pragma unroll
        for (int jj = 0; jj < 8; ++jj) wl[ks][jj] = LD64(rq, qo, qb0 + (32 * ks + jj) * (HQ * DH * 2), WQ_AUX);
        xa[ks] = LD128(rx, xo + ks * 64, 0, X_AUX);
      }
      *reinterpret_cast<u32x4*>(lds + L_FLAG) = u32x4{0u, 0u, 0u, 0u};
      *reinterpret_cast<u32x4*>(lds + L_FLAG + 4) = u32x4{0u, 0u, 0u, 0u};
      SB();
      BAR();
      READ_TAG();
      SB();
      f32x4 d[4];
#pragma unroll
      for (int i = 0; i < 4; ++i) d[i] = f32x4{0.f, 0.f, 0.f, 0.f};
#pragma unroll
      for (int ks = 0; ks < 4; ++ks)
#pragma unroll
        for (int i = 0; i < 4; ++i) {
          u32x4 bf;
#pragma unroll
          for (int p = 0; p < 4; ++p) bf[p] = __builtin_amdgcn_perm(wl[ks][2 * p + 1][i >> 1], wl[ks][2 * p][i >> 1], (i & 1) ? PERM_HI : PERM_LO);
          d[i] = mfma16(xa[ks], bf, d[i]);
        }
      const int half = g >> 1;
      const u32 qs = WS_QG + (u32)(((((4 * (g & 1) + 2 * half) * HKV + (qc >> 3)) * 4 + qk) * 512 + (qc & 7) * 64 + 2 * c) * 8);
#pragma unroll
      for (int e = 0; e < 2; ++e)
#pragma unroll
        for (int ih = 0; ih < 2; ++ih) {
          const float v0 = half ? d[2 * ih][2 + e] : d[2 * ih][e], v1 = half ? d[2 * ih + 1][2 + e] : d[2 * ih + 1][e];
          u32x4 gr = {__float_as_uint(v0), tag, __float_as_uint(v1), tag};
          __builtin_amdgcn_raw_buffer_store_b128(gr, rw, qs + ih * 256, e * (HKV * 4 * 512 * 8), QS_AUX);
        }
      LDS_ADD(F_PUB);
      STAMP(1);
    } else {
      BAR();
      READ_TAG();
      LDS_WAIT(F_PUB, 1, 2);
    }
    __builtin_amdgcn_s_sleep(QDELAY);
    {
      const u32 qoff = WS_QG + (u32)((xc * HKV + r2) * (4 * 512 * 8) + w * 1024 + lane * 16);
      u32x4 ga[4];
#pragma unroll
      for (int kq = 0; kq < 4; ++kq) ga[kq] = LD128(rw, qoff, kq * 4096, QL_AUX);
#pragma unroll 1
      for (u32 spin = 0; spin < SPIN_CAP; ++spin) {
        bool ok[4];
#pragma unroll
        for (int kq = 0; kq < 4; ++kq) ok[kq] = (ga[kq][1] == tag) & (ga[kq][3] == tag);
        COUNT(15);
        if (__all(ok[0] & ok[1] & ok[2] & ok[3])) break;
        MEMBAR();
#pragma unroll
        for (int kq = 0; kq < 4; ++kq) if (!ok[kq]) ga[kq] = LD128(rw, qoff, kq * 4096, QL_AUX);
      }
      const float q0 = (__uint_as_float(ga[0][0]) + __uint_as_float(ga[1][0])) + (__uint_as_float(ga[2][0]) + __uint_as_float(ga[3][0]));
      const float q1 = (__uint_as_float(ga[0][2]) + __uint_as_float(ga[1][2])) + (__uint_as_float(ga[2][2]) + __uint_as_float(ga[3][2]));
      lds[L_Q + 64 * w + 32 * (lane >> 5) + 2 * c + (g & 1)] = pack_bf16(q0, q1);
      LDS_ADD(F_Q);
      STAMP(2);
    }
    u32x4 Wf[4];
    {
      const __amdgpu_buffer_rsrc_t ro = __builtin_amdgcn_make_buffer_rsrc((void*)wo, 0, DM * HQ * DH * 2, RSRC_W3);
      const u32 wof = (u32)((32 * g * DM + 4 * c) * 2);
      const u32 wob = (u32)(((r * DH + 2 * w) * DM + 64 * xc) * 2);
      u32x2 W[8];
#pragma unroll
      for (int jj = 0; jj < 8; ++jj) W[jj] = LD64(ro, wof + (jj >> 2) * (DM * 2), wob + 8 * (jj & 3) * (DM * 2), WO_AUX);
#pragma unroll
      for (int i = 0; i < 4; ++i) {
#pragma unroll
        for (int p = 0; p < 4; ++p) Wf[i][p] = __builtin_amdgcn_perm(W[2 * p + 1][i >> 1], W[2 * p][i >> 1], (i & 1) ? PERM_HI : PERM_LO);
        asm volatile("" : "+v"(Wf[i]));
      }
    }
    if (w == 1) {
      LDS_WAIT(F_SM, 4, 8);
      const float mw = ldf[L_MAX + c], lw = ldf[L_SUM + c];
      float M = fmaxf(mw, dppf<0xB1>(mw)); M = fmaxf(M, dppf<0x4E>(M));
      float L = __builtin_amdgcn_exp2f(mw - M) * lw; L += dppf<0xB1>(L); L += dppf<0x4E>(L);
      const u32 mo = WS_ML + (u32)(((xc * HKV + r2) * 16 + c) * 16);
      if (lane < 16 && (lane & 3) == s) __builtin_amdgcn_raw_buffer_store_b128(u32x4{__float_as_uint(M), tag, __float_as_uint(L), tag}, rw, mo, 0, AUX_SC1);
      u32x4 ml;
#pragma unroll 1
      for (u32 spin = 0; spin < SPIN_CAP; ++spin) {
        __builtin_amdgcn_s_sleep(16);
        MEMBAR();
        ml = LD128(rw, mo, 0, AUX_SC1);
        COUNT(12);
        if (__all((ml[1] == tag) & (ml[3] == tag))) break;
      }
      const float ms = __uint_as_float(ml[0]), ls = __uint_as_float(ml[2]);
      float MG = fmaxf(ms, dppf<0xB1>(ms)); MG = fmaxf(MG, dppf<0x4E>(MG));
      float LG = __builtin_amdgcn_exp2f(ms - MG) * ls; LG += dppf<0xB1>(LG); LG += dppf<0x4E>(LG);
      if (lane < 16) ldf[L_FW + lane] = __builtin_amdgcn_exp2f(mw - MG) / LG;
      LDS_ADD(F_PV);
      STAMP(5);
    }
    const u32 go = WS_OG + (u32)((((r * NB + (c & 7)) * 4 + 2 * (c >> 3)) * 64 + 16 * w + 2 * g) * 8);
    const u32 pga = WS_PG + (u32)(((((4 * (lane >> 5) + 2 * (lane & 1)) * 32 + xc * 4 + w) * HQ + r) * 16 + ((lane >> 1) & 15)) * 8);
    const int lo = L_PC + w * 256 + 2 * lane;
    LDS_WAIT(F_VI, 4, 8);
    STAMP(7);
    u32x4 a[2][2];
#pragma unroll
    for (int sp = 0; sp < 2; ++sp) { a[sp][0] = LD128(rw, go + sp * 512, 0, AUX_SC1); a[sp][1] = LD128(rw, go + sp * 512 + 64, 0, AUX_SC1); }
#pragma unroll 1
    for (u32 spin = 0; spin < SPIN_CAP; ++spin) {
      const bool ok0 = (a[0][0][1] == tag) & (a[0][0][3] == tag) & (a[0][1][1] == tag) & (a[0][1][3] == tag);
      const bool ok1 = (a[1][0][1] == tag) & (a[1][0][3] == tag) & (a[1][1][1] == tag) & (a[1][1][3] == tag);
      COUNT(14);
      const int miss = __builtin_popcountll(__ballot(!ok0)) + __builtin_popcountll(__ballot(!ok1));
      if (miss == 0) break;
      STAMP(6);
      if (miss > 4 * CFAST) __builtin_amdgcn_s_sleep(CSLEEP);
      MEMBAR();
      if (!ok0) { a[0][0] = LD128(rw, go, 0, AUX_SC1); a[0][1] = LD128(rw, go + 64, 0, AUX_SC1); }
      if (!ok1) { a[1][0] = LD128(rw, go + 512, 0, AUX_SC1); a[1][1] = LD128(rw, go + 512 + 64, 0, AUX_SC1); }
    }
    STAMP(8);
    const u32x4 A0 = {a[0][0][0], a[0][0][2], a[0][1][0], a[0][1][2]}, A1 = {a[1][0][0], a[1][0][2], a[1][1][0], a[1][1][2]};
#pragma unroll
    for (int i = 0; i < 4; ++i) {
      f32x4 d = mfma16(A0, Wf[i], f32x4{0.f, 0.f, 0.f, 0.f});
      d = mfma16(A1, Wf[i], d);
      *reinterpret_cast<f32x4*>(ldf + L_PC + (w * 4 + i) * 256 + lane * 4) = d;
    }
    BAR();
    f32x2 t0 = {0.f, 0.f}, t1 = {0.f, 0.f};
#pragma unroll
    for (int kq = 0; kq < 4; ++kq) { t0 += *reinterpret_cast<const f32x2*>(ldf + lo + kq * 1024); t1 += *reinterpret_cast<const f32x2*>(ldf + lo + kq * 1024 + 128); }
    t0 += t1;
    __builtin_amdgcn_raw_buffer_store_b64(u32x2{__float_as_uint(t0[0]), tag}, rw, pga, 0, AUX_SC1);
    __builtin_amdgcn_raw_buffer_store_b64(u32x2{__float_as_uint(t0[1]), tag}, rw, pga, 32 * HQ * 16 * 8, AUX_SC1);
    STAMP(10);
    if (w == 0) {
      const __amdgpu_buffer_rsrc_t rout = __builtin_amdgcn_make_buffer_rsrc((void*)out, 0, NB * DM * 2, RSRC_W3);
      const u32 po = WS_PG + (u32)((xc * 32 + r) * (HQ * 16 * 8) + lane * 16);
      u32x4 pv[4];
#pragma unroll
      for (int i = 0; i < 4; ++i) pv[i] = LD128(rw, po + i * 1024, 0, AUX_SC1);
#pragma unroll 1
      for (u32 spin = 0; spin < SPIN_CAP; ++spin) {
        bool ok[4];
#pragma unroll
        for (int i = 0; i < 4; ++i) ok[i] = (pv[i][1] == tag) & (pv[i][3] == tag);
        COUNT(13);
        if (__all(ok[0] & ok[1] & ok[2] & ok[3])) break;
        STAMP(9);
        MEMBAR();
#pragma unroll
        for (int i = 0; i < 4; ++i) if (!ok[i]) pv[i] = LD128(rw, po + i * 1024, 0, AUX_SC1);
      }
      float a0 = (__uint_as_float(pv[0][0]) + __uint_as_float(pv[1][0])) + (__uint_as_float(pv[2][0]) + __uint_as_float(pv[3][0]));
      float a1 = (__uint_as_float(pv[0][2]) + __uint_as_float(pv[1][2])) + (__uint_as_float(pv[2][2]) + __uint_as_float(pv[3][2]));
      a0 += dppf<0x128>(a0); a1 += dppf<0x128>(a1);
      a0 = xg_sum(a0); a1 = xg_sum(a1);
      const u32 pk = pack_bf16(a0, a1);
      const int cc = 2 * (lane & 7) + ((lane >> 3) & 1);
      if (lane < 16) __builtin_amdgcn_raw_buffer_store_b16((u16)((lane & 8) ? (pk >> 16) : pk), rout, (u32)((xc * DM + 64 * r2 + 4 * cc + s) * 2), 0, 0);
      if (j == 0 && lane == 0) __builtin_amdgcn_raw_buffer_store_b32(epoch + 1u, rw, WS_EPOCH, 0, AUX_SC1);
      STAMP(11);
    }
  }
  STAMP_OUT();
}

extern "C" void kernel_launch(void* const* d_in, const int* in_sizes, int n_in, void* d_out, int out_size, void* d_ws, size_t ws_size,
                              hipStream_t stream) {
  (void)in_sizes; (void)n_in; (void)out_size; (void)ws_size;
  hipLaunchKernelGGL(gqa_decode_fused, dim3(256), dim3(512), 0, stream, (const u16*)d_in[0], (const u16*)d_in[1], (const u16*)d_in[2],
                     (const u16*)d_in[3], (const u16*)d_in[4], (u16*)d_out, (unsigned char*)d_ws + (ws_size / 2));
}
```

```cpp
#include <hip/hip_runtime.h>
#include <hip/hip_bf16.h>
#include <stdint.h>

typedef unsigned u32;
typedef unsigned short u16;
typedef u32 u32x4 __attribute__((ext_vector_type(4)));
typedef u32 u32x2 __attribute__((ext_vector_type(2)));
typedef float f32x4 __attribute__((ext_vector_type(4)));
typedef float f32x2 __attribute__((ext_vector_type(2)));
typedef __bf16 bf16x8 __attribute__((ext_vector_type(8)));
typedef __bf16 bf16x2 __attribute__((ext_vector_type(2)));
typedef short s16x4 __attribute__((ext_vector_type(4)));
#define LAS __attribute__((address_space(3)))

#define WIN 8
#define NKB 11
#define PACE 1370
#define PACEA 1700
#define PACEN 32
#define PACE0 8
#define PACET 130
#define PACEG 2
#define T0OFF 15
#define QDELAY 16
#define CDELAY 14
#define RDELAY 18
#define GQA_STAMP 0

constexpr int NB = 8, DM = 512, HQ = 32, HKV = 8, SK = 4096, DH = 128;
constexpr int AUX_SC1 = 16, AUX_NT = 2;
constexpr u32 RSRC_W3 = 0x00020000u;
constexpr u32 SPIN_CAP = 1u << 16;

constexpr u32 WS_EPOCH = 0;
constexpr u32 WS_T0 = 1024;
constexpr u32 WS_QG = 4096;
constexpr u32 WS_ML = WS_QG + NB * HKV * 4 * 256 * 8;
constexpr u32 WS_OG = WS_ML + NB * HKV * 16 * 16;
constexpr u32 WS_PG = WS_OG + HQ * NB * 4 * 64 * 8;
constexpr u32 WS_ST = WS_PG + NB * HQ * DM * 8;
constexpr u32 WS_BYTES = WS_ST + 256 * 8 * 16 * 8;

constexpr int L_PC = 0;
constexpr int L_Q = L_PC + 4096;
constexpr int L_FLAG = L_Q + 256;
constexpr int L_MAX = L_FLAG + 4;
constexpr int L_SUM = L_MAX + 16;
constexpr int L_ACC = L_SUM + 16;
constexpr int L_WORDS = L_ACC + 4 * 4 * 132;
constexpr int F_Q = 0, F_SM = 1, F_VI = 2, F_T0 = 3;

__device__ __forceinline__ f32x4 mfma16(u32x4 a, u32x4 b, f32x4 c) {
  return __builtin_amdgcn_mfma_f32_16x16x32_bf16(__builtin_bit_cast(bf16x8, a), __builtin_bit_cast(bf16x8, b), c, 0, 0, 0);
}
__device__ __forceinline__ u32 pack_bf16(float lo, float hi) {
  f32x2 f = {lo, hi};
  return __builtin_bit_cast(u32, __builtin_convertvector(f, bf16x2));
}
__device__ __forceinline__ float xg_max(float x) {
  auto s = __builtin_amdgcn_permlane16_swap(__float_as_uint(x), __float_as_uint(x), false, false);
  x = fmaxf(__uint_as_float(s[0]), __uint_as_float(s[1]));
  auto t = __builtin_amdgcn_permlane32_swap(__float_as_uint(x), __float_as_uint(x), false, false);
  return fmaxf(__uint_as_float(t[0]), __uint_as_float(t[1]));
}
__device__ __forceinline__ float xg_sum(float x) {
  auto s = __builtin_amdgcn_permlane16_swap(__float_as_uint(x), __float_as_uint(x), false, false);
  x = __uint_as_float(s[0]) + __uint_as_float(s[1]);
  auto t = __builtin_amdgcn_permlane32_swap(__float_as_uint(x), __float_as_uint(x), false, false);
  return __uint_as_float(t[0]) + __uint_as_float(t[1]);
}
template <int CTRL> __device__ __forceinline__ float dppf(float x) {
  return __builtin_bit_cast(float, __builtin_amdgcn_mov_dpp(__builtin_bit_cast(int, x), CTRL, 0xf, 0xf, true));
}
template <int N> __device__ __forceinline__ void wait_vm() { asm volatile("s_waitcnt vmcnt(%0)" :: "n"(N) : "memory"); }
__device__ __forceinline__ void pace_until(u32 t) {
  asm volatile("1:\n\ts_memrealtime vcc\n\ts_waitcnt lgkmcnt(0)\n\ts_sub_u32 vcc_lo, vcc_lo, %0\n\ts_cmpk_ge_u32 vcc_lo, 0x1000\n\ts_cbranch_scc1 2f\n\ts_sleep 1\n\ts_branch 1b\n2:"
               :: "s"(t - 4096u) : "memory", "scc", "vcc");
}
#define SB() __builtin_amdgcn_sched_barrier(0)
#define MEMBAR() asm volatile("" ::: "memory")
#define BAR() do { asm volatile("s_waitcnt lgkmcnt(0)" ::: "memory"); __builtin_amdgcn_s_barrier(); asm volatile("" ::: "memory"); } while (0)
#define LD128(rs, vo, so, aux) __builtin_amdgcn_raw_buffer_load_b128((rs), (vo), (so), (aux))
#define LD64(rs, vo, so, aux) __builtin_amdgcn_raw_buffer_load_b64((rs), (vo), (so), (aux))
#define READ_TAG() do { asm volatile("s_load_dword %0, %1, 0x0\n\ts_waitcnt lgkmcnt(0)" : "=s"(epoch) : "s"(ws) : "memory"); \
    tag = epoch * 0x9E3779B1u + 0x7F4A7C15u; tag ^= tag >> 15; tag *= 0x85EBCA77u; tag ^= tag >> 13; \
    tq0 = (epoch + 0x5bu) & 0xffu; tq1 = ((epoch >> 8) + epoch * 3u + 0xa7u) & 0xffu; } while (0)
#define LDS_ADD(i) do { asm volatile("s_waitcnt lgkmcnt(0)" ::: "memory"); \
    if (lane == 0) (void)__hip_atomic_fetch_add((LAS u32*)(lds + L_FLAG + (i)), 1u, __ATOMIC_RELAXED, __HIP_MEMORY_SCOPE_WORKGROUP); } while (0)
#define LDS_WAIT(i, n, slp) do { _Pragma("unroll 1") for (u32 spin_ = 0; spin_ < (SPIN_CAP << 6); ++spin_) { \
      if (__builtin_amdgcn_readfirstlane(*(volatile LAS u32*)(lds + L_FLAG + (i))) >= (u32)(n)) break; \
      if (slp) __builtin_amdgcn_s_sleep(slp); } MEMBAR(); } while (0)
#define PERM_LO 0x05040100u
#define PERM_HI 0x07060302u

#define STAMP(i) do { } while (0)
#define COUNT(i) do { } while (0)
#define STAMP_OUT() do { } while (0)

__global__ void __launch_bounds__(512, 2)
gqa_decode_fused(const u16* __restrict__ x, const u16* __restrict__ kc, const u16* __restrict__ vc, const u16* __restrict__ wq,
                 const u16* __restrict__ wo, u16* __restrict__ out, unsigned char* ws) {
  __shared__ __attribute__((aligned(16))) u32 lds[L_WORDS];
  float* const ldf = reinterpret_cast<float*>(lds);
  const int tid = threadIdx.x, lane = tid & 63, w = __builtin_amdgcn_readfirstlane(tid >> 6);
  const int g = lane >> 4, c = lane & 15;
  const int j = blockIdx.x, xc = j & 7, r = j >> 3, r2 = r >> 2, s = r & 3;
  STAMP(0);
  u32 tp0 = (u32)__builtin_amdgcn_s_memrealtime();
  asm volatile("" :: "s"(x), "s"(kc), "s"(vc), "s"(wq), "s"(wo), "s"(out), "s"(ws), "s"(tp0));
  const __amdgpu_buffer_rsrc_t rw = __builtin_amdgcn_make_buffer_rsrc((void*)ws, 0, WS_BYTES, RSRC_W3);

  if (w >= 4) {
    const int u = w & 3;
    const __amdgpu_buffer_rsrc_t rk = __builtin_amdgcn_make_buffer_rsrc((void*)kc, 0, NB * HKV * SK * DH * 2, RSRC_W3);
    const __amdgpu_buffer_rsrc_t rv = __builtin_amdgcn_make_buffer_rsrc((void*)vc, 0, NB * HKV * SK * DH * 2, RSRC_W3);
    const u32 kvbase = (u32)(((xc * HKV + r2) * SK + s * 1024 + u * 256) * (DH * 2));
    const u32 koff = (u32)((4 * (c & 3) + ((c >> 2) & 1)) * 256 + (c >> 3) * 64 + g * 16);
    const u32 voff = (u32)(lane * 16);
    const u32 tph = (u32)(u * (PACE * PACEG / 400));
    tp0 += tph;
    u32x4 K[64], V[64];
#define ISSUE(n) do { if ((n) < 128) { if ((n) >= PACE0 && ((n) % PACEG) == 0) pace_until(tp0 + (u32)(PACET + ((n) < PACEN ? ((n) - PACE0) * PACEA : (PACEN - PACE0) * PACEA + ((n) - PACEN) * PACE) / 100)); \
      if ((n) >= WIN) wait_vm<WIN - 1>(); \
      if ((n) < 64) K[(n) & 63] = LD128(rk, koff + (((n) >> 1) & 1) * 512 + ((n) & 1) * 128, kvbase + ((n) >> 2) * 4096, AUX_NT); \
      else V[(n) & 63] = LD128(rv, voff + ((n) & 3) * 1024, kvbase + (((n) & 63) >> 2) * 4096, AUX_NT); SB(); } } while (0)
#define ISSUE4(n) do { ISSUE(n); ISSUE((n) + 1); ISSUE((n) + 2); ISSUE((n) + 3); } while (0)
    BAR();
#pragma unroll
    for (int n = 0; n < 4 * NKB; ++n) ISSUE(n);
    STAMP(3);
    LDS_WAIT(F_Q, 1, 2);
    tp0 = __builtin_amdgcn_readfirstlane(lds[L_FLAG + F_T0]) + tph;
    STAMP(4);
    u32x4 Qf[2];
#pragma unroll
    for (int ln = 0; ln < 2; ++ln) Qf[ln] = *reinterpret_cast<const u32x4*>(lds + L_Q + (c & 3) * 64 + ln * 32 + ((c >> 2) & 1) * 16 + g * 4);
    f32x4 S[16];
    float mx = -INFINITY;
#define QK(t) do { f32x4 da = mfma16(K[4 * (t)], Qf[0], f32x4{0.f, 0.f, 0.f, 0.f}), db = mfma16(K[4 * (t) + 2], Qf[0], f32x4{0.f, 0.f, 0.f, 0.f}); \
      da = mfma16(K[4 * (t) + 1], Qf[1], da); db = mfma16(K[4 * (t) + 3], Qf[1], db); \
      _Pragma("unroll") for (int e_ = 0; e_ < 4; ++e_) { \
        auto sw = __builtin_amdgcn_permlane32_swap(__float_as_uint(da[e_]), __float_as_uint(db[e_]), false, false); \
        S[t][e_] = __uint_as_float(sw[0]) + dppf<0x104>(__uint_as_float(sw[1])); }     \
      mx = fmaxf(fmaxf(mx, fmaxf(S[t][0], S[t][1])), fmaxf(S[t][2], S[t][3])); SB(); } while (0)
    {
      constexpr int NP = NKB - 4;
      static_assert(2 * NP <= 16 && 4 * NKB + 4 * (NP - 1) - WIN >= 8 * NP, "paired tiles have landed");
#pragma unroll
      for (int i = 0; i < NP; ++i) { QK(2 * i); QK(2 * i + 1); ISSUE4(4 * NKB + 4 * i); }
#pragma unroll
      for (int t = 2 * NP; t < 16; ++t) { QK(t); ISSUE4(4 * NKB + 4 * (t - NP)); }
    }
    constexpr int N1 = 4 * NKB + 4 * (16 - (NKB - 4));
    constexpr float C2 = 0.088388347648318440f * 1.4426950408889634f;
    mx = xg_max(mx) * C2;
    float lsum = 0.f;
    u32x4 P[8];
#pragma unroll
    for (int t = 0; t < 16; ++t) {
      float p[4];
#pragma unroll
      for (int i = 0; i < 4; ++i) { p[i] = __builtin_amdgcn_exp2f(fmaf(S[t][i], C2, -mx)); lsum += p[i]; }
      P[t >> 1][(t & 1) * 2 + 0] = pack_bf16(p[0], p[1]);
      P[t >> 1][(t & 1) * 2 + 1] = pack_bf16(p[2], p[3]);
      if ((t & 3) == 3) { SB(); ISSUE(N1 + (t >> 2)); }
    }
    lsum = xg_sum(lsum);
    if (lane < 4) { ldf[L_MAX + lane * 4 + u] = mx; ldf[L_SUM + lane * 4 + u] = lsum; }
    LDS_ADD(F_SM);
    STAMP(5);
    constexpr int N2 = N1 + 4, LA = WIN > 9 ? WIN : 9;
    f32x4 acc[8];
#pragma unroll
    for (int i = 0; i < 8; ++i) acc[i] = f32x4{0.f, 0.f, 0.f, 0.f};
#pragma unroll
    for (int q = 0; q < 7; ++q) {
#pragma unroll
      for (int n = (q == 0 ? N2 : (64 + 8 * (q - 1) + 7 + LA + 4 > N2 ? 64 + 8 * (q - 1) + 7 + LA + 4 : N2)); n < 64 + 8 * q + 7 + LA; ++n) ISSUE(n);
      if (64 + 8 * q + 7 + LA >= 128 && 64 + 8 * (q - 1) + 7 + LA < 128) LDS_ADD(F_VI);
#pragma unroll
      for (int i = 0; i < 8; ++i) {
        u32x4 a_;
#pragma unroll
        for (int p = 0; p < 4; ++p) a_[p] = __builtin_amdgcn_perm(V[8 * q + 2 * p + 1][i >> 1], V[8 * q + 2 * p][i >> 1], (i & 1) ? PERM_HI : PERM_LO);
        acc[i] = mfma16(a_, P[q], acc[i]);
        if ((i & 1) && 64 + 8 * q + 7 + LA + (i >> 1) >= N2) { SB(); ISSUE(64 + 8 * q + 7 + LA + (i >> 1)); }
      }
      SB();
    }
    static_assert(64 + 8 * 6 + 7 + LA >= 128, "every request is out before the last step");
    {
      u32x4 a_[8];
#pragma unroll
      for (int p = 0; p < 4; ++p) {
#pragma unroll
        for (int i = 0; i < 8; ++i) a_[i][p] = __builtin_amdgcn_perm(V[56 + 2 * p + 1][i >> 1], V[56 + 2 * p][i >> 1], (i & 1) ? PERM_HI : PERM_LO);
        if (p < 3) SB();
      }
#pragma unroll
      for (int i = 0; i < 8; ++i) acc[i] = mfma16(a_[i], P[7], acc[i]);
    }
    STAMP(6);
    if (c < 4) {
#pragma unroll
      for (int i = 0; i < 8; ++i) *reinterpret_cast<f32x4*>(ldf + L_ACC + (u * 4 + c) * 132 + i * 16 + g * 4) = acc[i];
    }
    BAR();
  } else {
    u32 epoch, tag, tq0, tq1;
    __builtin_amdgcn_s_setprio(2);
    if (w == 0) {
      const int qk = xc & 3, qc = (xc >> 2) * 32 + r;
      const __amdgpu_buffer_rsrc_t rx = __builtin_amdgcn_make_buffer_rsrc((void*)x, 0, NB * DM * 2, RSRC_W3);
      const __amdgpu_buffer_rsrc_t rq = __builtin_amdgcn_make_buffer_rsrc((void*)wq, 0, DM * HQ * DH * 2, RSRC_W3);
      u32x2 wl[4][8];
      u32x4 xa[4];
      const u32 xo = (u32)(((c & 7) * DM + 128 * qk + 8 * g) * 2);
      const u32 qo = (u32)(8 * g * (HQ * DH * 2) + c * 8), qb0 = (u32)((128 * qk * (HQ * DH) + 64 * qc) * 2);
#pragma unroll
      for (int ks = 0; ks < 4; ++ks) {
#pragma unroll
        for (int jj = 0; jj < 8; ++jj) wl[ks][jj] = LD64(rq, qo, qb0 + (32 * ks + jj) * (HQ * DH * 2), AUX_NT);
        xa[ks] = LD128(rx, xo + ks * 64, 0, 0);
      }
      *reinterpret_cast<u32x4*>(lds + L_FLAG) = u32x4{0u, 0u, 0u, 0u};
      SB();
      BAR();
      READ_TAG();
      SB();
      f32x4 d[4];
#pragma unroll
      for (int i = 0; i < 4; ++i) d[i] = f32x4{0.f, 0.f, 0.f, 0.f};
#pragma unroll
      for (int ks = 0; ks < 4; ++ks)
#pragma unroll
        for (int i = 0; i < 4; ++i) {
          u32x4 bf;
#pragma unroll
          for (int p = 0; p < 4; ++p) bf[p] = __builtin_amdgcn_perm(wl[ks][2 * p + 1][i >> 1], wl[ks][2 * p][i >> 1], (i & 1) ? PERM_HI : PERM_LO);
          d[i] = mfma16(xa[ks], bf, d[i]);
        }
      const int half = g >> 1;
      const u32 qs = WS_QG + (u32)(((((4 * (g & 1) + 2 * half) * HKV + (qc >> 3)) * 4 + qk) * 256 + (qc & 7) * 32 + 2 * c) * 8);
#pragma unroll
      for (int e = 0; e < 2; ++e) {
        u32x4 gr;
#pragma unroll
        for (int i = 0; i < 4; ++i) gr[i] = ((__float_as_uint(half ? d[i][2 + e] : d[i][e]) + 0x80u) & 0xffffff00u) | ((i & 1) ? tq1 : tq0);
        __builtin_amdgcn_raw_buffer_store_b128(gr, rw, qs, e * (HKV * 4 * 256 * 8), AUX_SC1);
      }
      STAMP(1);
      __builtin_amdgcn_s_sleep(QDELAY);
      const u32 qoff = WS_QG + (u32)((xc * HKV + r2) * (4 * 256 * 8) + lane * 16);
      u32 vz; asm volatile("v_mov_b32 %0, 0" : "=v"(vz));
      const u32x4 t0g = LD128(rw, vz + WS_T0, 0, AUX_SC1);
      u32x4 ga[8];
#pragma unroll
      for (int i = 0; i < 8; ++i) ga[i] = LD128(rw, qoff + (i & 3) * 1024, (i >> 2) * 4096, AUX_SC1);
#pragma unroll 1
      for (u32 spin = 0; spin < SPIN_CAP; ++spin) {
        bool ok[8], all = true;
#pragma unroll
        for (int i = 0; i < 8; ++i) { ok[i] = (((ga[i][0] ^ tq0) | (ga[i][1] ^ tq1) | (ga[i][2] ^ tq0) | (ga[i][3] ^ tq1)) & 0xffu) == 0u; all &= ok[i]; }
        COUNT(15);
        if (__all(all)) break;
        __builtin_amdgcn_s_sleep(4);
        MEMBAR();
#pragma unroll
        for (int i = 0; i < 8; ++i) if (!ok[i]) ga[i] = LD128(rw, qoff + (i & 3) * 1024, (i >> 2) * 4096, AUX_SC1);
      }
#pragma unroll
      for (int e = 0; e < 2; ++e) {
        float qv[4];
#pragma unroll
        for (int i = 0; i < 4; ++i) qv[i] = (__uint_as_float(ga[e][i] & 0xffffff00u) + __uint_as_float(ga[2 + e][i] & 0xffffff00u)) + (__uint_as_float(ga[4 + e][i] & 0xffffff00u) + __uint_as_float(ga[6 + e][i] & 0xffffff00u));
        *reinterpret_cast<u32x2*>(lds + L_Q + 128 * e + lane * 2) = u32x2{pack_bf16(qv[0], qv[1]), pack_bf16(qv[2], qv[3])};
      }
      {
        const u32 tc = t0g[0] + T0OFF;
        const bool use = (t0g[1] == tag) & (t0g[3] == tag) & (tc - tp0 + 100u < 200u);
        lds[L_FLAG + F_T0] = use ? tc : tp0;
      }
      LDS_ADD(F_Q);
      STAMP(2);
    } else {
      BAR();
      READ_TAG();
      if (j == 0 && w == 1 && lane == 0) __builtin_amdgcn_raw_buffer_store_b128(u32x4{tp0, tag, tp0, tag}, rw, WS_T0, 0, AUX_SC1);
    }
    LDS_WAIT(F_SM, 4, 8);
    float fw[4];
    {
      const float mw = ldf[L_MAX + 4 * w + (lane & 3)], lw = ldf[L_SUM + 4 * w + (lane & 3)];
      float M = fmaxf(mw, dppf<0xB1>(mw)); M = fmaxf(M, dppf<0x4E>(M));
      float L = __builtin_amdgcn_exp2f(mw - M) * lw; L += dppf<0xB1>(L); L += dppf<0x4E>(L);
      const u32 mo = WS_ML + (u32)((((xc * HKV + r2) * 4 + w) * 4 + (lane & 3)) * 16);
      if (lane == s) __builtin_amdgcn_raw_buffer_store_b128(u32x4{__float_as_uint(M), tag, __float_as_uint(L), tag}, rw, mo, 0, AUX_SC1);
      u32x4 ml;
#pragma unroll 1
      for (u32 spin = 0; spin < SPIN_CAP; ++spin) {
        __builtin_amdgcn_s_sleep(40);
        MEMBAR();
        ml = LD128(rw, mo, 0, AUX_SC1);
        COUNT(12);
        if (__all((ml[1] == tag) & (ml[3] == tag))) break;
      }
      const float ms = __uint_as_float(ml[0]), ls = __uint_as_float(ml[2]);
      float MG = fmaxf(ms, dppf<0xB1>(ms)); MG = fmaxf(MG, dppf<0x4E>(MG));
      float LG = __builtin_amdgcn_exp2f(ms - MG) * ls; LG += dppf<0xB1>(LG); LG += dppf<0x4E>(LG);
      const float f = __builtin_amdgcn_exp2f(mw - MG) / LG;
#pragma unroll
      for (int q = 0; q < 4; ++q) { fw[q] = __builtin_bit_cast(float, __builtin_amdgcn_readlane(__builtin_bit_cast(int, f), q)); asm volatile("" : "+s"(fw[q])); }
    }
    STAMP(5);
    u32 oga = WS_OG + (u32)(((((r2 * 4 + w) * NB + xc) * 4 + s) * 64 + lane) * 8);
    u32 go = WS_OG + (u32)((((r * NB + (c & 7)) * 4 + 2 * (c >> 3)) * 64 + 16 * w + 2 * g) * 8);
    u32 pga = WS_PG + (u32)(((((4 * (lane >> 5) + 2 * (lane & 1)) * 32 + xc * 4 + w) * HQ + r) * 16 + ((lane >> 1) & 15)) * 8);
    asm volatile("" : "+v"(oga), "+v"(go), "+v"(pga));
    const int lo = L_PC + w * 256 + 2 * lane;
    LDS_WAIT(F_VI, 4, 2);
    u32x2 W[8];
    {
      const __amdgpu_buffer_rsrc_t ro = __builtin_amdgcn_make_buffer_rsrc((void*)wo, 0, DM * HQ * DH * 2, RSRC_W3);
      const u32 wof = (u32)((32 * g * DM + 4 * c) * 2);
      const u32 wob = (u32)(((r * DH + 2 * w) * DM + 64 * xc) * 2);
#pragma unroll
      for (int jj = 0; jj < 8; ++jj) W[jj] = LD64(ro, wof + (jj >> 2) * (DM * 2), wob + 8 * (jj & 3) * (DM * 2), AUX_NT);
    }
    SB();
    BAR();
    {
      f32x2 o[4];
#pragma unroll
      for (int q = 0; q < 4; ++q) o[q] = *reinterpret_cast<const f32x2*>(ldf + L_ACC + (q * 4 + w) * 132 + 2 * lane);
      const f32x2 ov = (fw[0] * o[0] + fw[1] * o[1]) + (fw[2] * o[2] + fw[3] * o[3]);
      __builtin_amdgcn_raw_buffer_store_b64(u32x2{pack_bf16(ov[0], ov[1]), tag}, rw, oga, 0, AUX_SC1);
    }
    STAMP(7);
    SB();
    u32x4 Wf[4];
#pragma unroll
    for (int i = 0; i < 4; ++i) {
#pragma unroll
      for (int p = 0; p < 4; ++p) Wf[i][p] = __builtin_amdgcn_perm(W[2 * p + 1][i >> 1], W[2 * p][i >> 1], (i & 1) ? PERM_HI : PERM_LO);
      asm volatile("" : "+v"(Wf[i]));
    }
    __builtin_amdgcn_s_sleep(CDELAY);
    STAMP(3);
    u32x4 a[2][2];
#pragma unroll
    for (int sp = 0; sp < 2; ++sp) { a[sp][0] = LD128(rw, go + sp * 512, 0, AUX_SC1); a[sp][1] = LD128(rw, go + sp * 512 + 64, 0, AUX_SC1); }
#pragma unroll 1
    for (u32 spin = 0; spin < SPIN_CAP; ++spin) {
      const bool ok0 = (a[0][0][1] == tag) & (a[0][0][3] == tag) & (a[0][1][1] == tag) & (a[0][1][3] == tag);
      const bool ok1 = (a[1][0][1] == tag) & (a[1][0][3] == tag) & (a[1][1][1] == tag) & (a[1][1][3] == tag);
      COUNT(14);
      if (__all(ok0 & ok1)) break;
      STAMP(6);
      MEMBAR();
      if (!ok0) { a[0][0] = LD128(rw, go, 0, AUX_SC1); a[0][1] = LD128(rw, go + 64, 0, AUX_SC1); }
      if (!ok1) { a[1][0] = LD128(rw, go + 512, 0, AUX_SC1); a[1][1] = LD128(rw, go + 512 + 64, 0, AUX_SC1); }
    }
    STAMP(8);
    const u32x4 A0 = {a[0][0][0], a[0][0][2], a[0][1][0], a[0][1][2]}, A1 = {a[1][0][0], a[1][0][2], a[1][1][0], a[1][1][2]};
#pragma unroll
    for (int i = 0; i < 4; ++i) {
      f32x4 d = mfma16(A0, Wf[i], f32x4{0.f, 0.f, 0.f, 0.f});
      d = mfma16(A1, Wf[i], d);
      *reinterpret_cast<f32x4*>(ldf + L_PC + (w * 4 + i) * 256 + lane * 4) = d;
    }
    BAR();
    f32x2 t0 = {0.f, 0.f}, t1 = {0.f, 0.f};
#pragma unroll
    for (int kq = 0; kq < 4; ++kq) { t0 += *reinterpret_cast<const f32x2*>(ldf + lo + kq * 1024); t1 += *reinterpret_cast<const f32x2*>(ldf + lo + kq * 1024 + 128); }
    t0 += t1;
    __builtin_amdgcn_raw_buffer_store_b64(u32x2{__float_as_uint(t0[0]), tag}, rw, pga, 0, AUX_SC1);
    __builtin_amdgcn_raw_buffer_store_b64(u32x2{__float_as_uint(t0[1]), tag}, rw, pga, 32 * HQ * 16 * 8, AUX_SC1);
    STAMP(10);
    if (w == 0) {
      const __amdgpu_buffer_rsrc_t rout = __builtin_amdgcn_make_buffer_rsrc((void*)out, 0, NB * DM * 2, RSRC_W3);
      const u32 po = WS_PG + (u32)((xc * 32 + r) * (HQ * 16 * 8) + lane * 16);
      u32x4 pv[4];
      __builtin_amdgcn_s_sleep(RDELAY);
#pragma unroll
      for (int i = 0; i < 4; ++i) pv[i] = LD128(rw, po + i * 1024, 0, AUX_SC1);
#pragma unroll 1
      for (u32 spin = 0; spin < SPIN_CAP; ++spin) {
        bool ok[4];
#pragma unroll
        for (int i = 0; i < 4; ++i) ok[i] = (pv[i][1] == tag) & (pv[i][3] == tag);
        COUNT(13);
        if (__all(ok[0] & ok[1] & ok[2] & ok[3])) break;
        STAMP(9);
        MEMBAR();
#pragma unroll
        for (int i = 0; i < 4; ++i) if (!ok[i]) pv[i] = LD128(rw, po + i * 1024, 0, AUX_SC1);
      }
      float a0 = (__uint_as_float(pv[0][0]) + __uint_as_float(pv[1][0])) + (__uint_as_float(pv[2][0]) + __uint_as_float(pv[3][0]));
      float a1 = (__uint_as_float(pv[0][2]) + __uint_as_float(pv[1][2])) + (__uint_as_float(pv[2][2]) + __uint_as_float(pv[3][2]));
      a0 += dppf<0x128>(a0); a1 += dppf<0x128>(a1);
      a0 = xg_sum(a0); a1 = xg_sum(a1);
      const u32 pk = pack_bf16(a0, a1);
      const int cc = 2 * (lane & 7) + ((lane >> 3) & 1);
      if (lane < 16) __builtin_amdgcn_raw_buffer_store_b16((u16)((lane & 8) ? (pk >> 16) : pk), rout, (u32)((xc * DM + 64 * r2 + 4 * cc + s) * 2), 0, 0);
      if (j == 0 && lane == 0) __builtin_amdgcn_raw_buffer_store_b32(epoch + 1u, rw, WS_EPOCH, 0, AUX_SC1);
      STAMP(11);
    }
  }
  STAMP_OUT();
}

extern "C" void kernel_launch(void* const* d_in, const int* in_sizes, int n_in, void* d_out, int out_size, void* d_ws, size_t ws_size,
                              hipStream_t stream) {
  (void)in_sizes; (void)n_in; (void)out_size; (void)ws_size;
  hipLaunchKernelGGL(gqa_decode_fused, dim3(256), dim3(512), 0, stream, (const u16*)d_in[0], (const u16*)d_in[1], (const u16*)d_in[2],
                     (const u16*)d_in[3], (const u16*)d_in[4], (u16*)d_out, (unsigned char*)d_ws);
}
```

```cpp
#include <hip/hip_runtime.h>
#include <hip/hip_bf16.h>
#include <stdint.h>

typedef unsigned u32;
typedef unsigned short u16;
typedef u32 u32x4 __attribute__((ext_vector_type(4)));
typedef u32 u32x2 __attribute__((ext_vector_type(2)));
typedef float f32x4 __attribute__((ext_vector_type(4)));
typedef float f32x2 __attribute__((ext_vector_type(2)));
typedef __bf16 bf16x8 __attribute__((ext_vector_type(8)));
typedef __bf16 bf16x2 __attribute__((ext_vector_type(2)));
#define LAS __attribute__((address_space(3)))

constexpr int PACE0 = 6, PACET = 125;
constexpr int PACEA = 1500, PACEN = 32;
constexpr int PACEK = 1350, PACEM = 96;
constexpr int PACE = 1420;
constexpr int PACEC = 1150, SLACK = 8;
constexpr int WIN = 10;
constexpr int NKB = 12;
constexpr int ESN = 14, EST = 25;
constexpr int CONV0 = 32, XREC = 60;
constexpr int ACP1 = 58, ACP2 = 87, ADEL = 130, ATHR = 40, ACAP = 150, AINV = 60, ASLOW = 6;
constexpr int TWO = 35, TC = 154;
constexpr int QDELAY = 36, RDELAY = 13;

constexpr int NB = 8, DM = 512, HQ = 32, HKV = 8, SK = 4096, DH = 128;
constexpr int AUX_SC1 = 16, AUX_NT = 2;
constexpr u32 RSRC_W3 = 0x00020000u;
constexpr u32 SPIN_CAP = 1u << 18;

constexpr u32 WS_EPOCH = 0;
constexpr u32 WS_T0 = 1024;
constexpr u32 WS_LT = 2048;
constexpr u32 WS_QG = 4096;
constexpr u32 WS_ML = WS_QG + NB * HKV * 4 * 256 * 8;
constexpr u32 WS_OG = WS_ML + NB * HKV * 16 * 16;
constexpr u32 WS_PG = WS_OG + HQ * NB * 4 * 64 * 8;
constexpr u32 WS_BYTES = WS_PG + NB * HQ * DM * 4;

constexpr int L_PC = 0;
constexpr int L_Q = L_PC + 4096;
constexpr int L_FLAG = L_Q + 256;
constexpr int L_MAX = L_FLAG + 32;
constexpr int L_SUM = L_MAX + 16;
constexpr int L_ACC = L_SUM + 16;
constexpr int L_WORDS = L_ACC + 4 * 4 * 132;
constexpr int F_Q = 0, F_SM = 1, F_VI = 2, F_PUB = 3, F_T0 = 4, F_LT = 8, F_SH = 16;

__device__ __forceinline__ f32x4 mfma16(u32x4 a, u32x4 b, f32x4 c) {
  return __builtin_amdgcn_mfma_f32_16x16x32_bf16(__builtin_bit_cast(bf16x8, a), __builtin_bit_cast(bf16x8, b), c, 0, 0, 0);
}
__device__ __forceinline__ u32 pack_bf16(float lo, float hi) {
  f32x2 f = {lo, hi};
  return __builtin_bit_cast(u32, __builtin_convertvector(f, bf16x2));
}
__device__ __forceinline__ float xg_max(float x) {
  auto s = __builtin_amdgcn_permlane16_swap(__float_as_uint(x), __float_as_uint(x), false, false);
  x = fmaxf(__uint_as_float(s[0]), __uint_as_float(s[1]));
  auto t = __builtin_amdgcn_permlane32_swap(__float_as_uint(x), __float_as_uint(x), false, false);
  return fmaxf(__uint_as_float(t[0]), __uint_as_float(t[1]));
}
__device__ __forceinline__ float xg_sum(float x) {
  auto s = __builtin_amdgcn_permlane16_swap(__float_as_uint(x), __float_as_uint(x), false, false);
  x = __uint_as_float(s[0]) + __uint_as_float(s[1]);
  auto t = __builtin_amdgcn_permlane32_swap(__float_as_uint(x), __float_as_uint(x), false, false);
  return __uint_as_float(t[0]) + __uint_as_float(t[1]);
}
template <int CTRL> __device__ __forceinline__ float dppf(float x) {
  return __builtin_bit_cast(float, __builtin_amdgcn_mov_dpp(__builtin_bit_cast(int, x), CTRL, 0xf, 0xf, true));
}
__device__ __forceinline__ void pace(u32& vt, u32 sched, u32 gap) {
  asm volatile("s_add_u32 %0, %0, %2\n\ts_sub_u32 vcc_lo, %0, %1\n\ts_cmp_lt_i32 vcc_lo, 0\n\ts_cselect_b32 %0, %1, %0\n"
               "1:\n\ts_memrealtime vcc\n\ts_waitcnt lgkmcnt(0)\n\ts_sub_u32 vcc_hi, vcc_lo, %0\n\ts_add_u32 vcc_lo, vcc_hi, 0x1000\n\ts_cmpk_ge_u32 vcc_lo, 0x1000\n\ts_cbranch_scc1 2f\n\ts_sleep 1\n\ts_branch 1b\n"
               "2:\n\ts_sub_u32 vcc_hi, vcc_hi, %3\n\ts_max_i32 vcc_hi, vcc_hi, 0\n\ts_add_u32 %0, %0, vcc_hi\n\ts_waitcnt vmcnt(%4)"
               : "+s"(vt) : "s"(sched), "s"(gap), "n"(SLACK), "n"(WIN - 2) : "memory", "scc", "vcc");
}
#define SB() __builtin_amdgcn_sched_barrier(0)
#define MEMBAR() asm volatile("" ::: "memory")
#define BAR() do { asm volatile("s_waitcnt lgkmcnt(0)" ::: "memory"); __builtin_amdgcn_s_barrier(); asm volatile("" ::: "memory"); } while (0)
#define LD128(rs, vo, so, aux) __builtin_amdgcn_raw_buffer_load_b128((rs), (vo), (so), (aux))
#define LD64(rs, vo, so, aux) __builtin_amdgcn_raw_buffer_load_b64((rs), (vo), (so), (aux))
#define READ_TAG() do { asm volatile("s_load_dword %0, %1, 0x0\n\ts_waitcnt lgkmcnt(0)" : "=s"(epoch) : "s"(ws) : "memory"); \
    tag = epoch * 0x9E3779B1u + 0x7F4A7C15u; tag ^= tag >> 15; tag *= 0x85EBCA77u; tag ^= tag >> 13; \
    tq0 = (epoch + 0x5bu) & 0xffu; tq1 = ((epoch >> 8) + epoch * 3u + 0xa7u) & 0xffu; } while (0)
#define LDS_ADD(i) do { asm volatile("s_waitcnt lgkmcnt(0)" ::: "memory"); \
    if (lane == 0) (void)__hip_atomic_fetch_add((LAS u32*)(lds + L_FLAG + (i)), 1u, __ATOMIC_RELAXED, __HIP_MEMORY_SCOPE_WORKGROUP); } while (0)
#define LDS_WAIT(i, n, slp) do { _Pragma("unroll 1") for (u32 spin_ = 0; spin_ < (SPIN_CAP << 4); ++spin_) { \
      if (__builtin_amdgcn_readfirstlane(*(volatile LAS u32*)(lds + L_FLAG + (i))) >= (u32)(n)) break; \
      if (slp) __builtin_amdgcn_s_sleep(slp); } MEMBAR(); } while (0)
#define PERM_LO 0x05040100u
#define PERM_HI 0x07060302u

constexpr int sched(int n) {
  return PACET + (n < PACEN ? (n - PACE0) * PACEA : n < PACEM ? (PACEN - PACE0) * PACEA + (n - PACEN) * PACEK : (PACEN - PACE0) * PACEA + (PACEM - PACEN) * PACEK + (n - PACEM) * PACE) / 100;
}
template <int SLP> __device__ __forceinline__ void wait_until(u32 t) {
  asm volatile("s_branch 2f\n1:\n\ts_sleep %1\n2:\n\ts_memrealtime vcc\n\ts_waitcnt lgkmcnt(0)\n\ts_sub_u32 vcc_lo, vcc_lo, %0\n\ts_cmpk_ge_u32 vcc_lo, 0x1000\n\ts_cbranch_scc0 1b"
               :: "s"(t - 4096u), "n"(SLP) : "memory", "scc", "vcc");
}

__global__ void __launch_bounds__(512, 2)
gqa_decode_fused(const u16* __restrict__ x, const u16* __restrict__ kc, const u16* __restrict__ vc, const u16* __restrict__ wq,
                 const u16* __restrict__ wo, u16* __restrict__ out, unsigned char* ws) {
  __shared__ __attribute__((aligned(16))) u32 lds[L_WORDS];
  float* const ldf = reinterpret_cast<float*>(lds);
  const int tid = threadIdx.x, lane = tid & 63, w = __builtin_amdgcn_readfirstlane(tid >> 6);
  const int g = lane >> 4, c = lane & 15;
  const int j = blockIdx.x, xc = j & 7, r = j >> 3, r2 = r >> 2, s = r & 3;
  u32x4 cpf;
  {
    unsigned long long pc; u32 len;
    asm volatile("s_getpc_b64 %0\n.Lgqa_code_begin:\n\ts_mov_b32 %1, .Lgqa_code_end-.Lgqa_code_begin" : "=s"(pc), "=s"(len));
    const __amdgpu_buffer_rsrc_t rc = __builtin_amdgcn_make_buffer_rsrc((void*)(pc & ~15ull), 0, w == 7 ? len : 0u, RSRC_W3);
    cpf = LD128(rc, (u32)(lane * 16), (u32)(r * 1024), 0);
  }
  lds[L_FLAG + (lane & 31)] = 0u;
  asm volatile("s_waitcnt lgkmcnt(0)" :: "s"(x), "s"(kc), "s"(vc), "s"(wq), "s"(wo), "s"(out), "s"(ws) : "memory");
  u32 tp0 = (u32)__builtin_amdgcn_s_memrealtime();
  const __amdgpu_buffer_rsrc_t rw = __builtin_amdgcn_make_buffer_rsrc((void*)ws, 0, WS_BYTES, RSRC_W3);
  const int u = w & 3;
  const u32 kvbase = (u32)((xc * HKV + r2) * (SK * DH * 2)) + (u32)(((u & 1) << 13) | ((u >> 1) << 15) | (s << 18));
  const u32 koff = (u32)((4 * (c & 3) + ((c >> 2) & 1)) * 256 + (c >> 3) * 64 + g * 16);
#define TOFF(t) (kvbase + (u32)((((t) & 1) << 12) | ((((t) >> 1) & 1) << 14) | (((t) >> 2) << 16)))
  u32x4 K[64];
  {
    const __amdgpu_buffer_rsrc_t rk0 = __builtin_amdgcn_make_buffer_rsrc((void*)kc, 0, w >= 4 ? NB * HKV * SK * DH * 2 : 0, RSRC_W3);
#pragma unroll
    for (int n = 0; n < PACE0; ++n) K[n] = LD128(rk0, koff + ((n >> 1) & 1) * 512 + (n & 1) * 128, TOFF(n >> 2), AUX_NT);
    SB();
  }
  __builtin_amdgcn_s_barrier();
  MEMBAR();

  if (w >= 4) {
    const __amdgpu_buffer_rsrc_t rk = __builtin_amdgcn_make_buffer_rsrc((void*)kc, 0, NB * HKV * SK * DH * 2, RSRC_W3);
    const __amdgpu_buffer_rsrc_t rv = __builtin_amdgcn_make_buffer_rsrc((void*)vc, 0, NB * HKV * SK * DH * 2, RSRC_W3);
    const u32 voff = (u32)(lane * 16);
    const u32 tph = (u32)(u * (PACE / 200));
    tp0 += tph;
    u32 vt = tp0;
    u32x4 V[64];
#define ISSUE(n) do { if ((n) < 128) { \
      if ((n) >= CONV0 && ((n) & 7) == 0) { const u32x2 be_ = *(volatile LAS u32x2*)(lds + L_FLAG + F_T0); \
        u32 b_ = __builtin_amdgcn_readfirstlane(be_[0]); const u32 t_ = b_ + (u32)((__builtin_amdgcn_readfirstlane((int)be_[1]) * ((n) - 120)) >> 6) + tph; \
        asm("s_cmp_lg_u32 %1, 0\n\ts_cselect_b32 %0, %2, %0" : "+s"(tp0) : "s"(b_), "s"(t_) : "scc"); } \
      if (((n) & 1) == 0) \
        pace(vt, tp0 + (u32)sched(n), (u32)((n) * PACEC / 100 - ((n) - 2) * PACEC / 100)); \
      if ((n) == ESN) {   \
        asm volatile("s_waitcnt vmcnt(4)" ::: "memory"); vt = (u32)__builtin_amdgcn_s_memrealtime(); \
        const int lt_ = (int)(vt - tp0 - (u32)sched(n)), sh_ = lt_ > EST ? min(lt_, 150) : 0; tp0 += (u32)sh_; *(volatile LAS u32*)(lds + L_FLAG + F_SH + u) = (u32)sh_ + 1u; } \
      if ((n) >= 4 * NKB && (n) <= 88 && ((n) & 3) == 0) *(volatile LAS u32x2*)(lds + L_FLAG + F_LT + 2 * u) = u32x2{vt - tp0 - (u32)sched(n), tp0 + (u32)sched((n) + 4)}; \
      if ((n) < 64) K[(n) & 63] = LD128(rk, koff + (((n) >> 1) & 1) * 512 + ((n) & 1) * 128, TOFF((n) >> 2), AUX_NT); \
      else V[(n) & 63] = LD128(rv, voff + ((n) & 3) * 1024, TOFF(((n) & 63) >> 2), AUX_NT); SB(); } } while (0)
#define ISSUE4(n) do { ISSUE(n); ISSUE((n) + 1); ISSUE((n) + 2); ISSUE((n) + 3); } while (0)
#pragma unroll
    for (int n = PACE0; n < 4 * NKB; ++n) ISSUE(n);
    asm volatile("" :: "v"(cpf));
    LDS_WAIT(F_Q, 4, 2);
    u32x4 Qf[2];
#pragma unroll
    for (int ln = 0; ln < 2; ++ln) Qf[ln] = *reinterpret_cast<const u32x4*>(lds + L_Q + (c & 3) * 64 + ln * 32 + ((c >> 2) & 1) * 16 + g * 4);
    f32x4 S[16];
    float mx = -INFINITY;
#define QK(t) do { f32x4 da = mfma16(K[4 * (t)], Qf[0], f32x4{0.f, 0.f, 0.f, 0.f}), db = mfma16(K[4 * (t) + 2], Qf[0], f32x4{0.f, 0.f, 0.f, 0.f}); \
      da = mfma16(K[4 * (t) + 1], Qf[1], da); db = mfma16(K[4 * (t) + 3], Qf[1], db); \
      _Pragma("unroll") for (int e_ = 0; e_ < 4; ++e_) { \
        auto sw = __builtin_amdgcn_permlane32_swap(__float_as_uint(da[e_]), __float_as_uint(db[e_]), false, false); \
        S[t][e_] = __uint_as_float(sw[0]) + dppf<0x104>(__uint_as_float(sw[1])); }     \
      mx = fmaxf(fmaxf(mx, fmaxf(S[t][0], S[t][1])), fmaxf(S[t][2], S[t][3])); SB(); } while (0)
    {
      constexpr int NP = NKB - 4;
      static_assert(2 * NP <= 16 && 4 * NKB + 4 * (NP - 1) - WIN >= 8 * NP, "paired tiles have landed");
#pragma unroll
      for (int i = 0; i < NP; ++i) { QK(2 * i); QK(2 * i + 1); ISSUE4(4 * NKB + 4 * i); }
#pragma unroll
      for (int t = 2 * NP; t < 16; ++t) { QK(t); ISSUE4(4 * NKB + 4 * (t - NP)); }
    }
    constexpr int N1 = 4 * NKB + 4 * (16 - (NKB - 4));
    constexpr float C2 = 0.088388347648318440f * 1.4426950408889634f;
    mx = xg_max(mx) * C2;
    float lsum = 0.f;
    u32x4 P[8];
#pragma unroll
    for (int t = 0; t < 16; ++t) {
      float p[4];
#pragma unroll
      for (int i = 0; i < 4; ++i) { p[i] = __builtin_amdgcn_exp2f(fmaf(S[t][i], C2, -mx)); lsum += p[i]; }
      P[t >> 1][(t & 1) * 2 + 0] = pack_bf16(p[0], p[1]);
      P[t >> 1][(t & 1) * 2 + 1] = pack_bf16(p[2], p[3]);
      if (t & 1) { SB(); ISSUE(N1 + (t >> 1)); }
    }
    lsum = xg_sum(lsum);
    if (lane < 4) { ldf[L_MAX + lane * 4 + u] = mx; ldf[L_SUM + lane * 4 + u] = lsum; }
    LDS_ADD(F_SM);
    static_assert(N1 == 80, "the request plan below starts at 88");
    f32x4 acc[8];
#pragma unroll
    for (int i = 0; i < 8; ++i) acc[i] = f32x4{0.f, 0.f, 0.f, 0.f};
#pragma unroll
    for (int q = 0; q < 7; ++q) {
      const int pb = q < 4 ? 88 + 6 * q : 82 + 8 * q, pa = q == 0 ? 88 : q < 4 ? pb - 2 : pb - 4;
#pragma unroll
      for (int k = 0; k < 4; ++k) if (pa + k < pb) ISSUE(pa + k);
      if (q == 6) LDS_ADD(F_VI);
#pragma unroll
      for (int i = 0; i < 8; ++i) {
        u32x4 a_;
#pragma unroll
        for (int p = 0; p < 4; ++p) a_[p] = __builtin_amdgcn_perm(V[8 * q + 2 * p + 1][i >> 1], V[8 * q + 2 * p][i >> 1], (i & 1) ? PERM_HI : PERM_LO);
        acc[i] = mfma16(a_, P[q], acc[i]);
        if (i & 1) { SB(); ISSUE(pb + (i >> 1)); }
      }
      SB();
    }
    {
      u32x4 a_[8];
#pragma unroll
      for (int p = 0; p < 4; ++p) {
#pragma unroll
        for (int i = 0; i < 8; ++i) a_[i][p] = __builtin_amdgcn_perm(V[56 + 2 * p + 1][i >> 1], V[56 + 2 * p][i >> 1], (i & 1) ? PERM_HI : PERM_LO);
        if (p < 3) SB();
      }
#pragma unroll
      for (int i = 0; i < 8; ++i) acc[i] = mfma16(a_[i], P[7], acc[i]);
    }
    if (c < 4) {
#pragma unroll
      for (int i = 0; i < 8; ++i) *reinterpret_cast<f32x4*>(ldf + L_ACC + (u * 4 + c) * 132 + i * 16 + g * 4) = acc[i];
    }
    BAR();
    asm volatile(".Lgqa_code_end:");
  } else {
    u32 epoch, tag, tq0, tq1, sbase = 0, sextra = 0, sslow = 0;
#define OWN_SHIFT(dst) do { const u32 s_ = *(volatile LAS u32*)(lds + L_FLAG + F_SH + (lane & 3));     \
      int e_ = s_ ? (int)s_ - 1 : max((int)((u32)__builtin_amdgcn_s_memrealtime() - tp0) - sched(ESN) - 30, 0); \
      e_ += __builtin_amdgcn_mov_dpp(e_, 0xB1, 0xf, 0xf, true); e_ += __builtin_amdgcn_mov_dpp(e_, 0x4E, 0xf, 0xf, true); dst = (u32)min(__builtin_amdgcn_readfirstlane(e_) >> 2, 150); } while (0)
#define REPORT(k) do { if (j < 8) { const u32x2 l_ = *(volatile LAS u32x2*)(lds + L_FLAG + F_LT + 2 * (lane & 3));     \
        int m_ = max((int)l_[0], (int)((u32)__builtin_amdgcn_s_memrealtime() - (l_[1] ? l_[1] : tp0 + (u32)sched(4 * NKB)))); \
        m_ = max(m_, __builtin_amdgcn_mov_dpp(m_, 0xB1, 0xf, 0xf, true)); m_ = max(m_, __builtin_amdgcn_mov_dpp(m_, 0x4E, 0xf, 0xf, true)); \
        if (lane == 0) __builtin_amdgcn_raw_buffer_store_b128(u32x4{(u32)m_, tag, (u32)m_, tag}, rw, WS_LT + (k) * 256u + (u32)j * 16u, 0, AUX_SC1); } } while (0)
#define YIELD(gr, nk) do { int l_ = (((gr)[1] == tag) & ((gr)[3] == tag)) ? (int)min((gr)[0], (u32)ACAP) : AINV; \
      l_ = max(l_, __builtin_amdgcn_mov_dpp(l_, 0xB1, 0xf, 0xf, true)); l_ = max(l_, __builtin_amdgcn_mov_dpp(l_, 0x4E, 0xf, 0xf, true)); l_ = max(l_, __builtin_amdgcn_mov_dpp(l_, 0x141, 0xf, 0xf, true)); \
      l_ = __builtin_amdgcn_readfirstlane(l_); \
      if (l_ > ATHR) { sbase = (sbase + (u32)(l_ + ASLOW * (120 - (nk)) / 16)) | 1u; sextra += 4 * ASLOW; sslow += ASLOW; *(volatile LAS u32x2*)(lds + L_FLAG + F_T0) = u32x2{sbase, sextra}; } } while (0)
    {
      const int qk = xc & 3, qc = (xc >> 2) * 32 + r;
      const __amdgpu_buffer_rsrc_t rx = __builtin_amdgcn_make_buffer_rsrc((void*)x, 0, NB * DM * 2, RSRC_W3);
      const __amdgpu_buffer_rsrc_t rq = __builtin_amdgcn_make_buffer_rsrc((void*)wq, 0, DM * HQ * DH * 2, RSRC_W3);
      u32x2 wl[8];
      const u32 qo = (u32)(8 * g * (HQ * DH * 2) + c * 8), qb0 = (u32)(((128 * qk + 32 * w) * (HQ * DH) + 64 * qc) * 2);
#pragma unroll
      for (int jj = 0; jj < 8; ++jj) wl[jj] = LD64(rq, qo, qb0 + jj * (HQ * DH * 2), AUX_NT);
      const u32x4 xa = LD128(rx, (u32)(((c & 7) * DM + 8 * g) * 2), (u32)((128 * qk + 32 * w) * 2), 0);
      SB();
      __builtin_amdgcn_s_setprio(2);
      READ_TAG();
      SB();
      f32x4 d[4];
#pragma unroll
      for (int i = 0; i < 4; ++i) {
        u32x4 bf;
#pragma unroll
        for (int p = 0; p < 4; ++p) bf[p] = __builtin_amdgcn_perm(wl[2 * p + 1][i >> 1], wl[2 * p][i >> 1], (i & 1) ? PERM_HI : PERM_LO);
        d[i] = mfma16(xa, bf, f32x4{0.f, 0.f, 0.f, 0.f});
      }
#pragma unroll
      for (int e = 0; e < 2; ++e) {
        f32x4 v;
#pragma unroll
        for (int i = 0; i < 4; ++i) v[i] = (g >> 1) ? d[i][2 + e] : d[i][e];
        *reinterpret_cast<f32x4*>(ldf + L_PC + w * 512 + (4 * (g & 1) + 2 * (g >> 1) + e) * 64 + 4 * c) = v;
      }
      LDS_ADD(F_PUB);
      const u32 qs = WS_QG + (u32)(((((2 * w + (lane >> 5)) * HKV + (qc >> 3)) * 4 + qk) * 256 + (qc & 7) * 32 + (lane & 31)) * 8);
      LDS_WAIT(F_PUB, 4, 0);
      f32x2 pq[4];
#pragma unroll
      for (int q = 0; q < 4; ++q) pq[q] = *reinterpret_cast<const f32x2*>(ldf + L_PC + q * 512 + w * 128 + 2 * lane);
      const f32x2 ps = (pq[0] + pq[1]) + (pq[2] + pq[3]);
      __builtin_amdgcn_raw_buffer_store_b64(u32x2{((__float_as_uint(ps[0]) + 0x80u) & 0xffffff00u) | tq0, ((__float_as_uint(ps[1]) + 0x80u) & 0xffffff00u) | tq1}, rw, qs, 0, AUX_SC1);
      if (j < 8 && w == 1) {
        wait_until<2>(tp0 + (u32)(sched(ESN) + 35));
        u32 sh; OWN_SHIFT(sh);
        if (lane == 0) __builtin_amdgcn_raw_buffer_store_b128(u32x4{tp0 + sh, tag, tp0 + sh, tag}, rw, WS_T0 + (u32)j * 16u, 0, AUX_SC1);
      }
      __builtin_amdgcn_s_sleep(QDELAY);
      const u32 qoff = WS_QG + (u32)((xc * HKV + r2) * (4 * 256 * 8) + w * 512 + lane * 8);
      u32x2 ga[4];
#pragma unroll
      for (int i = 0; i < 4; ++i) ga[i] = LD64(rw, qoff, i * 2048, AUX_SC1);
#pragma unroll 1
      for (u32 spin = 0; spin < SPIN_CAP; ++spin) {
        bool ok[4];
#pragma unroll
        for (int i = 0; i < 4; ++i) ok[i] = (((ga[i][0] ^ tq0) | (ga[i][1] ^ tq1)) & 0xffu) == 0u;
        if (__all(ok[0] & ok[1] & ok[2] & ok[3])) break;
        __builtin_amdgcn_s_sleep(4);
        MEMBAR();
#pragma unroll
        for (int i = 0; i < 4; ++i) if (__any(!ok[i])) ga[i] = LD64(rw, qoff, i * 2048, AUX_SC1);
      }
      const float q0 = (__uint_as_float(ga[0][0] & 0xffffff00u) + __uint_as_float(ga[1][0] & 0xffffff00u)) + (__uint_as_float(ga[2][0] & 0xffffff00u) + __uint_as_float(ga[3][0] & 0xffffff00u));
      const float q1 = (__uint_as_float(ga[0][1] & 0xffffff00u) + __uint_as_float(ga[1][1] & 0xffffff00u)) + (__uint_as_float(ga[2][1] & 0xffffff00u) + __uint_as_float(ga[3][1] & 0xffffff00u));
      lds[L_Q + 64 * w + lane] = pack_bf16(q0, q1);
      LDS_ADD(F_Q);
      if (w == 2 && s == 0) {
        u32 sh, t0, t1; OWN_SHIFT(sh);
        if (sh >= 8u) asm volatile("s_load_dword %0, %2, 0x0\n\ts_load_dword %1, %3, 0x0\n\ts_waitcnt lgkmcnt(0)" : "=&s"(t0), "=&s"(t1)
                     : "s"(vc + (size_t)(xc * HKV + r2) * SK * DH), "s"(wo + (size_t)(4 * xc + (r >> 3)) * DH * DM + 64 * (r & 7)) : "memory");
      }
      if (w == 0) {
        MEMBAR();
        u32x4 t0g = LD128(rw, WS_T0 + (u32)(lane & 7) * 16u, 0, AUX_SC1);
        { u32 sh; OWN_SHIFT(sh); tp0 += sh; }
#pragma unroll 1
        for (int rep = 0; rep < 2 && !__all((t0g[1] == tag) & (t0g[3] == tag)); ++rep) {
          __builtin_amdgcn_s_sleep(8);
          MEMBAR();
          t0g = LD128(rw, WS_T0 + (u32)(lane & 7) * 16u, 0, AUX_SC1);
        }
        int dt = (int)(t0g[0] - tp0);
        dt = ((t0g[1] == tag) & (t0g[3] == tag) & ((u32)(dt + 250) < 500u)) ? dt : 0;
        int dm = max(dt, __builtin_amdgcn_mov_dpp(dt, 0xB1, 0xf, 0xf, true)); dm = max(dm, __builtin_amdgcn_mov_dpp(dm, 0x4E, 0xf, 0xf, true)); dm = max(dm, __builtin_amdgcn_mov_dpp(dm, 0x141, 0xf, 0xf, true));
        dt += __builtin_amdgcn_mov_dpp(dt, 0xB1, 0xf, 0xf, true); dt += __builtin_amdgcn_mov_dpp(dt, 0x4E, 0xf, 0xf, true); dt += __builtin_amdgcn_mov_dpp(dt, 0x141, 0xf, 0xf, true);
        dt = max(dt, 8 * (dm - XREC));
        dt = __builtin_amdgcn_readfirstlane(dt);
        sbase = (tp0 + (u32)(dt >> 3)) | 1u;
        sextra = (u32)(dt * 8 / (120 - CONV0));
        *(volatile LAS u32x2*)(lds + L_FLAG + F_T0) = u32x2{sbase, sextra};
      }
    }
    if (w == 0) {
      wait_until<4>(sbase + (u32)sched(ACP1));
      REPORT(0);
      wait_until<4>(sbase + (u32)(sched(ACP1) + ADEL));
      MEMBAR();
      const u32x4 lg = LD128(rw, WS_LT + (u32)(lane & 7) * 16u, 0, AUX_SC1);
      YIELD(lg, 76);
    }
    const u32 tcp2 = sbase - ((sslow * 36u) >> 4) + (u32)sched(ACP2);
    if (w == 0 && j < 8) { wait_until<4>(tcp2); REPORT(1); }
    LDS_WAIT(F_SM, 4, 8);
    float fw[4];
    {
      const float mw = ldf[L_MAX + 4 * w + (lane & 3)], lw = ldf[L_SUM + 4 * w + (lane & 3)];
      float M = fmaxf(mw, dppf<0xB1>(mw)); M = fmaxf(M, dppf<0x4E>(M));
      float L = __builtin_amdgcn_exp2f(mw - M) * lw; L += dppf<0xB1>(L); L += dppf<0x4E>(L);
      const u32 mo = WS_ML + (u32)((((xc * HKV + r2) * 4 + w) * 4 + (lane & 3)) * 16);
      if (lane == s) __builtin_amdgcn_raw_buffer_store_b128(u32x4{__float_as_uint(M), tag, __float_as_uint(L), tag}, rw, mo, 0, AUX_SC1);
      if (w == 0) {
        wait_until<4>(tcp2 + ADEL);
        MEMBAR();
        const u32x4 lg = LD128(rw, WS_LT + 256u + (u32)(lane & 7) * 16u, 0, AUX_SC1);
        YIELD(lg, 104);
      }
      u32x4 ml;
#pragma unroll 1
      for (u32 spin = 0; spin < SPIN_CAP; ++spin) {
        if (w != 0 || spin != 0) __builtin_amdgcn_s_sleep(40);
        MEMBAR();
        ml = LD128(rw, mo, 0, AUX_SC1);
        if (__all((ml[1] == tag) & (ml[3] == tag))) break;
      }
      const float ms = __uint_as_float(ml[0]), ls = __uint_as_float(ml[2]);
      float MG = fmaxf(ms, dppf<0xB1>(ms)); MG = fmaxf(MG, dppf<0x4E>(MG));
      float LG = __builtin_amdgcn_exp2f(ms - MG) * ls; LG += dppf<0xB1>(LG); LG += dppf<0x4E>(LG);
      const float f = __builtin_amdgcn_exp2f(mw - MG) / LG;
#pragma unroll
      for (int q = 0; q < 4; ++q) { fw[q] = __builtin_bit_cast(float, __builtin_amdgcn_readlane(__builtin_bit_cast(int, f), q)); asm volatile("" : "+s"(fw[q])); }
    }
    u32 oga = WS_OG + (u32)((((((r2 * 4 + w) * 8 + (lane >> 3)) * 2 + (s & 1)) * 16 + (s >> 1) * 8 + xc) * 8 + (lane & 7)) * 8);
    const int hd = 4 * xc + (r >> 3), cs = r & 7;
    u32 go = WS_OG + (u32)((hd * 8 + 2 * w) * 2048 + c * 64 + g * 16);
    u32 pga = WS_PG + (u32)(((((2 * (lane >> 5) + (lane & 1)) * 64 + (cs * 4 + w) * 2 + ((lane >> 4) & 1)) * HQ + hd) * 8 + ((lane >> 1) & 7)) * 8);
    asm volatile("" : "+v"(oga), "+v"(go), "+v"(pga));
    const int lo = L_PC + w * 256 + 2 * lane;
    LDS_WAIT(F_VI, 4, 2);
    const u32 tb = __builtin_amdgcn_readfirstlane(*(volatile LAS u32*)(lds + L_FLAG + F_T0)) + (u32)sched(126);
    wait_until<0>(tb + TWO);
    u32x2 W[8];
    {
      const __amdgpu_buffer_rsrc_t ro = __builtin_amdgcn_make_buffer_rsrc((void*)wo, 0, DM * HQ * DH * 2, RSRC_W3);
      const u32 wof = (u32)((32 * g * DM + 4 * c) * 2);
      const u32 wob = (u32)(((hd * DH + 2 * w) * DM + 64 * cs) * 2);
#pragma unroll
      for (int jj = 0; jj < 8; ++jj) W[jj] = LD64(ro, wof + (jj >> 2) * (DM * 2), wob + 8 * (jj & 3) * (DM * 2), AUX_NT);
    }
    SB();
    BAR();
    {
      f32x2 o[4];
#pragma unroll
      for (int q = 0; q < 4; ++q) o[q] = *reinterpret_cast<const f32x2*>(ldf + L_ACC + (q * 4 + w) * 132 + 2 * lane);
      const f32x2 ov = (fw[0] * o[0] + fw[1] * o[1]) + (fw[2] * o[2] + fw[3] * o[3]);
      __builtin_amdgcn_raw_buffer_store_b64(u32x2{pack_bf16(ov[0], ov[1]), tag}, rw, oga, 0, AUX_SC1);
    }
    SB();
    u32x4 Wf[4];
#pragma unroll
    for (int i = 0; i < 4; ++i) {
#pragma unroll
      for (int p = 0; p < 4; ++p) Wf[i][p] = __builtin_amdgcn_perm(W[2 * p + 1][i >> 1], W[2 * p][i >> 1], (i & 1) ? PERM_HI : PERM_LO);
      asm volatile("" : "+v"(Wf[i]));
    }
    __builtin_amdgcn_s_sleep(2);
    wait_until<0>(tb + TC);
    u32x4 a[2][2];
#pragma unroll
    for (int sp = 0; sp < 2; ++sp) { a[sp][0] = LD128(rw, go + sp * 1024, 0, AUX_SC1); a[sp][1] = LD128(rw, go + sp * 1024 + 2048, 0, AUX_SC1); }
#pragma unroll 1
    for (u32 spin = 0; spin < SPIN_CAP; ++spin) {
      const u32x4 A0 = {a[0][0][0], a[0][0][2], a[0][1][0], a[0][1][2]}, A1 = {a[1][0][0], a[1][0][2], a[1][1][0], a[1][1][2]};
#pragma unroll
      for (int i = 0; i < 4; ++i) {
        f32x4 d = mfma16(A0, Wf[i], f32x4{0.f, 0.f, 0.f, 0.f});
        d = mfma16(A1, Wf[i], d);
        *reinterpret_cast<f32x4*>(ldf + L_PC + (w * 4 + i) * 256 + lane * 4) = d;
      }
      const u32 bad0 = (a[0][0][1] ^ tag) | (a[0][0][3] ^ tag) | (a[0][1][1] ^ tag) | (a[0][1][3] ^ tag);
      const u32 bad1 = (a[1][0][1] ^ tag) | (a[1][0][3] ^ tag) | (a[1][1][1] ^ tag) | (a[1][1][3] ^ tag);
      if (__builtin_amdgcn_ballot_w64((bad0 | bad1) != 0u) == 0ull) break;
      MEMBAR();
      if (bad0) { a[0][0] = LD128(rw, go, 0, AUX_SC1); a[0][1] = LD128(rw, go + 2048, 0, AUX_SC1); }
      if (bad1) { a[1][0] = LD128(rw, go + 1024, 0, AUX_SC1); a[1][1] = LD128(rw, go + 3072, 0, AUX_SC1); }
    }
    BAR();
    f32x2 tk[4];
#pragma unroll
    for (int kq = 0; kq < 4; ++kq) tk[kq] = *reinterpret_cast<const f32x2*>(ldf + lo + kq * 1024) + *reinterpret_cast<const f32x2*>(ldf + lo + kq * 1024 + 128);
    const f32x2 t0 = (tk[0] + tk[1]) + (tk[2] + tk[3]);
    __builtin_amdgcn_raw_buffer_store_b64(u32x2{((__float_as_uint(t0[0]) + 0x80u) & 0xffffff00u) | tq0, ((__float_as_uint(t0[1]) + 0x80u) & 0xffffff00u) | tq1}, rw, pga, 0, AUX_SC1);
    if (w == 0) {
      const __amdgpu_buffer_rsrc_t rout = __builtin_amdgcn_make_buffer_rsrc((void*)out, 0, NB * DM * 2, RSRC_W3);
      const u32 po = WS_PG + (u32)(j * (HQ * 8 * 8) + lane * 16);
      u32 oo = (u32)(((2 * (j >> 6) + ((lane >> 2) & 1)) * DM + 64 * ((j >> 3) & 7) + 4 * (8 * (j & 1) + 2 * (lane & 3) + ((lane >> 3) & 1)) + ((j >> 1) & 3)) * 2);
      if (lane >= 16) oo = 0xfffffff0u;
      asm volatile("" : "+v"(oo));
      u32x4 pv[2];
      __builtin_amdgcn_s_sleep(RDELAY);
#pragma unroll
      for (int i = 0; i < 2; ++i) pv[i] = LD128(rw, po + i * 1024, 0, AUX_SC1);
      float tot;
#pragma unroll 1
      for (u32 spin = 0; spin < SPIN_CAP; ++spin) {
        float a[4];
#pragma unroll
        for (int i = 0; i < 4; ++i) {
          a[i] = __uint_as_float(pv[0][i] & 0xffffff00u) + __uint_as_float(pv[1][i] & 0xffffff00u);
          a[i] += dppf<0x128>(a[i]);
          a[i] += dppf<0x124>(a[i]);
        }
        const float b01 = (lane & 4) ? a[1] : a[0], b23 = (lane & 4) ? a[3] : a[2];
        tot = xg_sum((lane & 8) ? b23 : b01);
        asm volatile("" : "+v"(tot));
        u32 bad = 0;
#pragma unroll
        for (int i = 0; i < 2; ++i) bad |= (pv[i][0] ^ tq0) | (pv[i][1] ^ tq1) | (pv[i][2] ^ tq0) | (pv[i][3] ^ tq1);
        if (__builtin_amdgcn_ballot_w64((bad & 0xffu) != 0u) == 0ull) break;
        MEMBAR();
#pragma unroll
        for (int i = 0; i < 2; ++i) pv[i] = LD128(rw, po + i * 1024, 0, AUX_SC1);
      }
      __builtin_amdgcn_raw_buffer_store_b16((u16)pack_bf16(tot, tot), rout, oo, 0, 0);
      if (j == 0 && lane == 0) __builtin_amdgcn_raw_buffer_store_b32(epoch + 1u, rw, WS_EPOCH, 0, AUX_SC1);
    }
  }
}

extern "C" void kernel_launch(void* const* d_in, const int* in_sizes, int n_in, void* d_out, int out_size, void* d_ws, size_t ws_size,
                              hipStream_t stream) {
  (void)in_sizes; (void)n_in; (void)out_size; (void)ws_size;
  hipLaunchKernelGGL(gqa_decode_fused, dim3(256), dim3(512), 0, stream, (const u16*)d_in[0], (const u16*)d_in[1], (const u16*)d_in[2],
                     (const u16*)d_in[3], (const u16*)d_in[4], (u16*)d_out, (unsigned char*)d_ws);
}
```
